```python
import torch

def _softmax(x, axis=-1):
    return torch.softmax(x, dim=axis)
def _gelu(x):
    return torch.nn.functional.gelu(x, approximate="tanh")
def _silu(x):
    return torch.nn.functional.silu(x)
def _sigmoid(x):
    return torch.sigmoid(x)
def _layernorm(x, axis=-1, eps=1e-5):
    m = x.mean(dim=axis, keepdim=True)
    v = x.var(dim=axis, keepdim=True, unbiased=False)
    return (x - m) / torch.sqrt(v + eps)
def _scatter_add(tgt, idx, src, axis=0):
    return tgt.clone().index_add_(axis, idx, src)
def _segment_sum(src, seg, n):
    out = torch.zeros((n,) + src.shape[1:], dtype=src.dtype)
    return out.index_add_(0, seg, src)
def _pad(x, pw):
    flat = tuple(v for pair in reversed(pw) for v in pair)
    return torch.nn.functional.pad(x, flat)

kernel_name = 'gated_mix__mha_block_cached__swiglu_ffn__decode'

def setup_inputs():

    _w = (lambda a: (lambda t: t.to(torch.bfloat16) if t.is_floating_point() else t)(torch.as_tensor(a)))
    def _A_setup():
        B, Sq, D = 8, 1, 448
        H, Sk, Dh = 16, 4096, 256
        return {
            "x": _w((lambda *s: torch.randn(*s, dtype=torch.bfloat16) * 0.05)(B, Sq, D)),
            "k_cache": _w((lambda *s: torch.randn(*s, dtype=torch.bfloat16) * 6.4)(B, H, Sk, Dh)),
            "v_cache": _w((lambda *s: torch.randn(*s, dtype=torch.bfloat16) * 0.2)(B, H, Sk, Dh)),
            "wq": _w((lambda *s: torch.randn(*s, dtype=torch.bfloat16) * 6.4)(D, H * Dh) * (D ** -0.5)),
            "wo": _w((lambda *s: torch.randn(*s, dtype=torch.bfloat16) * 0.2)(H * Dh, D) * ((H * Dh) ** -0.5)),
        }
    _A_in = _A_setup()

    def _B_setup():
        B, S, D, F = 8, 1, 448, 1792
        return {
            "x": _w((lambda *s: torch.randn(*s, dtype=torch.bfloat16) * 0.05)(B, S, D)),
            "w_gate": _w((lambda *s: torch.randn(*s, dtype=torch.bfloat16) * 0.05)(D, F)),
            "w_up": _w((lambda *s: torch.randn(*s, dtype=torch.bfloat16) * 0.05)(D, F)),
            "w_down": _w((lambda *s: torch.randn(*s, dtype=torch.bfloat16) * 0.05)(F, D)),
        }
    _B_in = _B_setup()

    return {
        "A_x": _A_in["x"],
        "A_k_cache": _A_in["k_cache"],
        "A_v_cache": _A_in["v_cache"],
        "A_wq": _A_in["wq"],
        "A_wo": _A_in["wo"],
        "B_x": _B_in["x"],
        "B_w_gate": _B_in["w_gate"],
        "B_w_up": _B_in["w_up"],
        "B_w_down": _B_in["w_down"],
        "gate_w": _w((lambda *s: torch.randn(*s, dtype=torch.bfloat16) * 0.05)(448) * 16.0),
    }

def reference(A_x, A_k_cache, A_v_cache, A_wq, A_wo, B_x, B_w_gate, B_w_up, B_w_down, gate_w):
    def _A_ref(x, k_cache, v_cache, wq, wo):
        B, Sq, D = x.shape
        H, Dh = 16, 256
        q = (lambda x, *d: x.permute(*d))((x @ wq).reshape(B, Sq, H, Dh), 0, 2, 1, 3)
        scores = torch.einsum("bhqd,bhkd->bhqk", q, k_cache) * (Dh ** -0.5)
        p = _softmax(scores, axis=-1)
        o = torch.einsum("bhqk,bhkd->bhqd", p, v_cache)
        return (lambda x, *d: x.permute(*d))(o, 0, 2, 1, 3).reshape(B, Sq, H * Dh) @ wo

    def _B_ref(x, w_gate, w_up, w_down):
        g = _silu(x @ w_gate)
        u = x @ w_up
        return (g * u) @ w_down

    x = A_x
    g = _sigmoid((x * gate_w).sum(-1, keepdims=True))
    a = _A_ref(x, A_k_cache, A_v_cache, A_wq, A_wo)
    b = _B_ref(x, B_w_gate, B_w_up, B_w_down)
    return g * a + (1 - g) * b
```

```cpp
#include <hip/hip_runtime.h>
#include <hip/hip_bf16.h>
#include <stdint.h>

typedef unsigned u32x4 __attribute__((ext_vector_type(4)));
typedef float f32x4 __attribute__((ext_vector_type(4)));
typedef __bf16 bf16x2 __attribute__((ext_vector_type(2)));
typedef unsigned short u16;

constexpr int NB = 8, NH = 16, SK = 4096, DH = 256, DM = 448, FF = 1792;
constexpr int NSPLIT = 2, KEYS_WG = SK / NSPLIT, NWAVE = 8, KEYS_W = KEYS_WG / NWAVE, NL = KEYS_W / 2;
constexpr int WIN = 16;
#define INTERLEAVE 0
constexpr int LSTRIDE = INTERLEAVE ? 8192 : 1024;
constexpr int WSTRIDE = INTERLEAVE ? 1024 : NL * 1024;

__device__ __forceinline__ float bf2f(u16 v) { return __uint_as_float((unsigned)v << 16); }
__device__ __forceinline__ float bflo(unsigned v) { return __uint_as_float(v << 16); }
__device__ __forceinline__ float bfhi(unsigned v) { return __uint_as_float(v & 0xffff0000u); }
__device__ __forceinline__ unsigned cvtpk(float lo, float hi) {
  unsigned r; asm("v_cvt_pk_bf16_f32 %0, %1, %2" : "=v"(r) : "v"(lo), "v"(hi)); return r;
}
__device__ __forceinline__ float dot2(unsigned k, unsigned q, float acc) {
  return __builtin_amdgcn_fdot2_f32_bf16(__builtin_bit_cast(bf16x2, k), __builtin_bit_cast(bf16x2, q), acc, false);
}
template <int CTRL> __device__ __forceinline__ float dpp(float x) {
  return __builtin_bit_cast(float, __builtin_amdgcn_mov_dpp(__builtin_bit_cast(int, x), CTRL, 0xf, 0xf, true));
}
constexpr int XOR1 = 0xB1, XOR2 = 0x4E, XOR7 = 0x141, XOR8 = 0x128, ROR4 = 0x124;
__device__ __forceinline__ float swap16_add(float a, float b) {
  auto s = __builtin_amdgcn_permlane16_swap(__float_as_uint(a), __float_as_uint(b), false, false);
  return __uint_as_float(s[0]) + __uint_as_float(s[1]);
}
__device__ __forceinline__ float swap32_add(float a, float b) {
  auto s = __builtin_amdgcn_permlane32_swap(__float_as_uint(a), __float_as_uint(b), false, false);
  return __uint_as_float(s[0]) + __uint_as_float(s[1]);
}
template <int CTRL, int BIT> __device__ __forceinline__ float tstep(float lo, float hi, int lane) {
  const float t1 = lo + dpp<CTRL>(lo), t2 = hi + dpp<CTRL>(hi);
  return (lane & BIT) ? t2 : t1;
}
__device__ __forceinline__ float wave_max(float x) {
  x = fmaxf(x, dpp<XOR1>(x)); x = fmaxf(x, dpp<XOR2>(x)); x = fmaxf(x, dpp<XOR7>(x)); x = fmaxf(x, dpp<XOR8>(x));
  auto s = __builtin_amdgcn_permlane16_swap(__float_as_uint(x), __float_as_uint(x), false, false);
  x = fmaxf(__uint_as_float(s[0]), __uint_as_float(s[1]));
  auto t = __builtin_amdgcn_permlane32_swap(__float_as_uint(x), __float_as_uint(x), false, false);
  return fmaxf(__uint_as_float(t[0]), __uint_as_float(t[1]));
}
__device__ __forceinline__ float wave_sum(float x) {
  x = x + dpp<XOR1>(x); x = x + dpp<XOR2>(x); x = x + dpp<XOR7>(x); x = x + dpp<XOR8>(x);
  auto s = __builtin_amdgcn_permlane16_swap(__float_as_uint(x), __float_as_uint(x), false, false);
  x = __uint_as_float(s[0]) + __uint_as_float(s[1]);
  auto t = __builtin_amdgcn_permlane32_swap(__float_as_uint(x), __float_as_uint(x), false, false);
  return __uint_as_float(t[0]) + __uint_as_float(t[1]);
}

constexpr size_t WS_Q = 0;
constexpr size_t WS_H = WS_Q + NB * NH * DH * 2;
constexpr size_t WS_PO = WS_H + NB * FF * 2;
constexpr size_t WS_ML = WS_PO + NB * NH * NSPLIT * DH * 4;

__global__ void __launch_bounds__(256) prep_kernel(const u16* x, const u16* wq, const u16* wg, const u16* wu, unsigned char* ws) {
  __shared__ float xs[NB][DM];
  const int tid = threadIdx.x;
  for (int i = tid; i < NB * DM; i += 256) xs[i / DM][i % DM] = bf2f(x[i]);
  __syncthreads();
  const int blk = blockIdx.x;
  if (blk < 16) {
    const int col = blk * 256 + tid;
    float acc[NB];
    for (int b = 0; b < NB; ++b) acc[b] = 0.f;
#pragma unroll 8
    for (int k = 0; k < DM; ++k) { const float w = bf2f(wq[(size_t)k * (NH * DH) + col]);
#pragma unroll
      for (int b = 0; b < NB; ++b) acc[b] = fmaf(xs[b][k], w, acc[b]); }
    u16* q = (u16*)(ws + WS_Q);
    for (int b = 0; b < NB; ++b) q[b * NH * DH + col] = (u16)(cvtpk(acc[b], 0.f) & 0xffff);
  } else {
    const int col = (blk - 16) * 256 + tid;
    float ag[NB], au[NB];
    for (int b = 0; b < NB; ++b) { ag[b] = 0.f; au[b] = 0.f; }
#pragma unroll 8
    for (int k = 0; k < DM; ++k) { const float w1 = bf2f(wg[(size_t)k * FF + col]), w2 = bf2f(wu[(size_t)k * FF + col]);
#pragma unroll
      for (int b = 0; b < NB; ++b) { ag[b] = fmaf(xs[b][k], w1, ag[b]); au[b] = fmaf(xs[b][k], w2, au[b]); } }
    u16* h = (u16*)(ws + WS_H);
    for (int b = 0; b < NB; ++b) { const float g = ag[b] / (1.f + __expf(-ag[b])); h[b * FF + col] = (u16)(cvtpk(g * au[b], 0.f) & 0xffff); }
  }
}

#define LDNT(rs, voff, soff) __builtin_amdgcn_raw_buffer_load_b128(rs, voff, soff, 2)

__global__ void __launch_bounds__(512, 2) attn_kernel(const u16* kc, const u16* vc, unsigned char* ws) {
  __shared__ __attribute__((aligned(16))) float lds[NWAVE * 256 + NWAVE * 256 + 32];
  const int tid = threadIdx.x, lane = tid & 63, w = __builtin_amdgcn_readfirstlane(tid >> 6);
  const int blk = blockIdx.x;
  const int j = blk & 31, b = blk >> 5, h = j & 15, s = j >> 4;
  const size_t base = ((size_t)(b * NH + h) * SK + (size_t)s * KEYS_WG) * DH;
  const __amdgpu_buffer_rsrc_t rk = __builtin_amdgcn_make_buffer_rsrc((void*)(kc + base), 0, KEYS_WG * DH * 2, 0x00020000);
  const __amdgpu_buffer_rsrc_t rv = __builtin_amdgcn_make_buffer_rsrc((void*)(vc + base), 0, KEYS_WG * DH * 2, 0x00020000);
  const int voff = lane * 16 + w * WSTRIDE;
  const u32x4 q = *(const u32x4*)(ws + WS_Q + ((size_t)(b * NH + h) * DH + (lane & 31) * 8) * 2);
  u32x4 a[WIN];
#pragma unroll
  for (int i = 0; i < WIN; ++i) a[i] = LDNT(rk, voff, i * LSTRIDE);
  __builtin_amdgcn_sched_barrier(0);
  float sc[4];
#pragma unroll
  for (int it = 0; it < 4; ++it) {
    float t1[16], t2, u2[8], u3[4], u4[2];
    float x0;
#pragma unroll
    for (int i = 0; i < 32; ++i) {
      const int n = it * 32 + i;
      const u32x4 cur = a[i % WIN];
      if (n + WIN < NL) a[i % WIN] = LDNT(rk, voff, (n + WIN) * LSTRIDE);
      else a[i % WIN] = LDNT(rv, voff, (n + WIN - NL) * LSTRIDE);
      float d = dot2(cur[0], q[0], 0.f); d = dot2(cur[1], q[1], d); d = dot2(cur[2], q[2], d); d = dot2(cur[3], q[3], d);
      if ((i & 1) == 0) x0 = d;
      else {
        t1[i >> 1] = swap16_add(x0, d);
        if ((i & 3) == 3) { u2[i >> 2] = tstep<XOR8, 8>(t1[(i >> 1) - 1], t1[i >> 1], lane);
          if ((i & 7) == 7) { u3[i >> 3] = tstep<XOR7, 4>(u2[(i >> 2) - 1], u2[i >> 2], lane);
            if ((i & 15) == 15) { u4[i >> 4] = tstep<XOR2, 2>(u3[(i >> 3) - 1], u3[i >> 3], lane);
              if (i == 31) sc[it] = tstep<XOR1, 1>(u4[0], u4[1], lane); } } }
      }
      __builtin_amdgcn_sched_barrier(0);
    }
    (void)t2;
  }
  const float C2 = 0.0625f * 1.4426950408889634f;
  float m = fmaxf(fmaxf(sc[0], sc[1]), fmaxf(sc[2], sc[3]));
  m = wave_max(m);
  float lsum = 0.f;
  const int il = ((lane >> 4) & 1) | (((lane >> 3) & 1) << 1) | (((lane >> 2) & 1) << 2) | (((lane >> 1) & 1) << 3) | ((lane & 1) << 4);
  float* pw = lds + w * 256 + (lane >> 5) * 128;
#pragma unroll
  for (int it = 0; it < 4; ++it) { const float p = __builtin_amdgcn_exp2f((sc[it] - m) * C2); lsum += p; pw[it * 32 + il] = p; }
  __builtin_amdgcn_wave_barrier();
  asm volatile("s_waitcnt lgkmcnt(0)" ::: "memory");
  float acc[8];
#pragma unroll
  for (int k = 0; k < 8; ++k) acc[k] = 0.f;
  f32x4 pv;
#pragma unroll
  for (int n = 0; n < NL; ++n) {
    if ((n & 3) == 0) pv = *(const f32x4*)(pw + n);
    const u32x4 cur = a[n % WIN];
    if (n + WIN < NL) a[n % WIN] = LDNT(rv, voff, (n + WIN) * LSTRIDE);
    const float p = pv[n & 3];
#pragma unroll
    for (int k = 0; k < 4; ++k) { acc[2 * k] = fmaf(p, bflo(cur[k]), acc[2 * k]); acc[2 * k + 1] = fmaf(p, bfhi(cur[k]), acc[2 * k + 1]); }
    __builtin_amdgcn_sched_barrier(0);
  }
  lsum = wave_sum(lsum);
  float* op = lds + NWAVE * 256;
  f32x4 r;
#pragma unroll
  for (int k = 0; k < 4; ++k) r[k] = swap32_add(acc[k], acc[k + 4]);
  *(f32x4*)(op + w * 256 + (lane & 31) * 8 + (lane >> 5) * 4) = r;
  float* ml = lds + 2 * NWAVE * 256;
  if (lane == 0) { ml[2 * w] = m; ml[2 * w + 1] = lsum; }
  __syncthreads();
  if (tid < 256) {
    float M = ml[0];
#pragma unroll
    for (int i = 1; i < NWAVE; ++i) M = fmaxf(M, ml[2 * i]);
    float L = 0.f, o = 0.f;
#pragma unroll
    for (int i = 0; i < NWAVE; ++i) { const float f = __builtin_amdgcn_exp2f((ml[2 * i] - M) * C2); L = fmaf(f, ml[2 * i + 1], L); o = fmaf(f, op[i * 256 + tid], o); }
    float* po = (float*)(ws + WS_PO); float* pml = (float*)(ws + WS_ML);
    const int slot = (b * NH + h) * NSPLIT + s;
    po[slot * DH + tid] = o;
    if (tid == 0) { pml[slot * 2] = M * C2; pml[slot * 2 + 1] = L; }
  }
}

__global__ void __launch_bounds__(448) final_kernel(const u16* x, const u16* wo, const u16* wd, const u16* gw, const unsigned char* ws, u16* out) {
  __shared__ float o[NH * DH];
  __shared__ float hs[FF];
  __shared__ float red[8];
  const int b = blockIdx.x, tid = threadIdx.x;
  const float* po = (const float*)(ws + WS_PO); const float* pml = (const float*)(ws + WS_ML);
  for (int i = tid; i < NH * DH; i += 448) { const int h = i / DH, d = i % DH; const int slot = (b * NH + h) * NSPLIT;
    const float m0 = pml[slot * 2], l0 = pml[slot * 2 + 1], m1 = pml[slot * 2 + 2], l1 = pml[slot * 2 + 3];
    const float M = fmaxf(m0, m1), f0 = exp2f(m0 - M), f1 = exp2f(m1 - M);
    const float v = (f0 * po[slot * DH + d] + f1 * po[(slot + 1) * DH + d]) / (f0 * l0 + f1 * l1);
    o[i] = bf2f((u16)(cvtpk(v, 0.f) & 0xffff)); }
  const u16* h = (const u16*)(ws + WS_H);
  for (int i = tid; i < FF; i += 448) hs[i] = bf2f(h[b * FF + i]);
  float gp = bf2f(x[b * DM + tid]) * bf2f(gw[tid]);
  gp = wave_sum(gp);
  if ((tid & 63) == 0) red[tid >> 6] = gp;
  __syncthreads();
  float gs = 0.f;
  for (int i = 0; i < 7; ++i) gs += red[i];
  const float g = 1.f / (1.f + __expf(-gs));
  float a = 0.f, f = 0.f;
#pragma unroll 16
  for (int k = 0; k < NH * DH; ++k) a = fmaf(o[k], bf2f(wo[(size_t)k * DM + tid]), a);
#pragma unroll 16
  for (int k = 0; k < FF; ++k) f = fmaf(hs[k], bf2f(wd[(size_t)k * DM + tid]), f);
  out[b * DM + tid] = (u16)(cvtpk(g * a + (1.f - g) * f, 0.f) & 0xffff);
}

extern "C" void kernel_launch(void* const* d_in, const int* in_sizes, int n_in, void* d_out, int out_size, void* d_ws, size_t ws_size, hipStream_t stream) {
  const u16* x = (const u16*)d_in[0]; const u16* kc = (const u16*)d_in[1]; const u16* vc = (const u16*)d_in[2];
  const u16* wq = (const u16*)d_in[3]; const u16* wo = (const u16*)d_in[4];
  const u16* wg = (const u16*)d_in[6]; const u16* wu = (const u16*)d_in[7]; const u16* wd = (const u16*)d_in[8]; const u16* gw = (const u16*)d_in[9];
  unsigned char* ws = (unsigned char*)d_ws;
  prep_kernel<<<16 + 7, 256, 0, stream>>>(x, wq, wg, wu, ws);
  attn_kernel<<<256, 512, 0, stream>>>(kc, vc, ws);
  final_kernel<<<8, 448, 0, stream>>>(x, wo, wd, gw, ws, (u16*)d_out);
}
```

```cpp
#include <hip/hip_runtime.h>
#include <hip/hip_bf16.h>
#include <stdint.h>

typedef unsigned u32x4 __attribute__((ext_vector_type(4)));
typedef unsigned u32x2 __attribute__((ext_vector_type(2)));
typedef float f32x4 __attribute__((ext_vector_type(4)));
typedef float f32x2 __attribute__((ext_vector_type(2)));
typedef short s16x4 __attribute__((ext_vector_type(4)));
typedef short s16x8 __attribute__((ext_vector_type(8)));
typedef __bf16 bf16x2 __attribute__((ext_vector_type(2)));
typedef __bf16 bf16x8 __attribute__((ext_vector_type(8)));
typedef unsigned short u16;
typedef unsigned long long u64;
#define GAS __attribute__((address_space(1)))
#define LAS __attribute__((address_space(3)))
#define RLX_AGENT __ATOMIC_RELAXED, __HIP_MEMORY_SCOPE_AGENT
#define RLX_WG __ATOMIC_RELAXED, __HIP_MEMORY_SCOPE_WORKGROUP

constexpr int NB = 8, NH = 16, SK = 4096, DH = 256, DM = 448, FF = 1792;
#define KF 5
#define QDELAY 10
#define KP 18
#define VX 2
#define HSLEEP 14
#define HSLEEP2 20
constexpr int F = KF, P = KP, R = 32;
constexpr int LS = 8192;
constexpr float C2 = 0.0625f * 1.4426950408889634f;
constexpr unsigned SPIN_CAP = 1u << 18;
constexpr int OOB = 0x7ff00000;

constexpr int WS_EPOCH = 0;
constexpr int WS_Q = 4096;
constexpr int WS_ML = WS_Q + 16 * 8 * 256 * 8;
constexpr int WS_Z = WS_ML + 256 * 128;
constexpr int WS_END = WS_Z + 8 * 32 * 32 * 256;
#define STAMP(k) do {} while (0)
#define STAMP_DECL do {} while (0)
#define STAMP_FLUSH do {} while (0)

constexpr int L_WS = 0;
constexpr int L_YP = 0;
constexpr int L_P = L_WS + 4 * 32 * 224;
constexpr int L_OP = L_P + 2048;
constexpr int L_FP = L_OP + 1024;
constexpr int L_QS = L_P;
constexpr int L_F1 = L_FP + 4 * 448;
constexpr int L_QR = L_F1 + 512;
constexpr int L_Q = L_QR + 512;
constexpr int L_FIN = L_Q + 128;
constexpr int L_ML = L_FIN + 128;
constexpr int L_FAC = L_ML + 8;
constexpr int L_CNT = L_FAC + 8;
constexpr int L_END = L_CNT + 8;
static_assert(L_QS + 4096 <= L_F1, "wq tiles must fit below L_F1");

struct Params {
  const u16 *x, *kc, *vc, *wq, *wo, *wg, *wu, *wd, *gw;
  u16* out;
  unsigned char* ws;
};

__device__ __forceinline__ float bflo(unsigned v) { return __uint_as_float(v << 16); }
__device__ __forceinline__ float bfhi(unsigned v) { return __uint_as_float(v & 0xffff0000u); }
__device__ __forceinline__ unsigned cvtpk(float lo, float hi) {
  unsigned r; asm("v_cvt_pk_bf16_f32 %0, %1, %2" : "=v"(r) : "v"(lo), "v"(hi)); return r;
}
__device__ __forceinline__ float dot2(unsigned k, unsigned q, float acc) {
  return __builtin_amdgcn_fdot2_f32_bf16(__builtin_bit_cast(bf16x2, k), __builtin_bit_cast(bf16x2, q), acc, false);
}
template <int CTRL> __device__ __forceinline__ float dpp(float x) {
  return __builtin_bit_cast(float, __builtin_amdgcn_mov_dpp(__builtin_bit_cast(int, x), CTRL, 0xf, 0xf, true));
}
constexpr int XOR1 = 0xB1, XOR2 = 0x4E, XOR7 = 0x141, XOR8 = 0x128;
__device__ __forceinline__ float swap16_add(float a, float b) {
  auto s = __builtin_amdgcn_permlane16_swap(__float_as_uint(a), __float_as_uint(b), false, false);
  return __uint_as_float(s[0]) + __uint_as_float(s[1]);
}
__device__ __forceinline__ float swap32_add(float a, float b) {
  auto s = __builtin_amdgcn_permlane32_swap(__float_as_uint(a), __float_as_uint(b), false, false);
  return __uint_as_float(s[0]) + __uint_as_float(s[1]);
}
template <int CTRL, int BIT> __device__ __forceinline__ float tstep(float lo, float hi, int lane) {
  const float t1 = lo + dpp<CTRL>(lo), t2 = hi + dpp<CTRL>(hi);
  return (lane & BIT) ? t2 : t1;
}
__device__ __forceinline__ float wave_max(float x) {
  x = fmaxf(x, dpp<XOR1>(x)); x = fmaxf(x, dpp<XOR2>(x)); x = fmaxf(x, dpp<XOR7>(x)); x = fmaxf(x, dpp<XOR8>(x));
  auto s = __builtin_amdgcn_permlane16_swap(__float_as_uint(x), __float_as_uint(x), false, false);
  x = fmaxf(__uint_as_float(s[0]), __uint_as_float(s[1]));
  auto t = __builtin_amdgcn_permlane32_swap(__float_as_uint(x), __float_as_uint(x), false, false);
  return fmaxf(__uint_as_float(t[0]), __uint_as_float(t[1]));
}
__device__ __forceinline__ float wave_sum(float x) {
  x = x + dpp<XOR1>(x); x = x + dpp<XOR2>(x); x = x + dpp<XOR7>(x); x = x + dpp<XOR8>(x);
  auto s = __builtin_amdgcn_permlane16_swap(__float_as_uint(x), __float_as_uint(x), false, false);
  x = __uint_as_float(s[0]) + __uint_as_float(s[1]);
  auto t = __builtin_amdgcn_permlane32_swap(__float_as_uint(x), __float_as_uint(x), false, false);
  return __uint_as_float(t[0]) + __uint_as_float(t[1]);
}
__device__ __forceinline__ void pair_mac(const u32x4& A, const u32x4& B, unsigned xp, float (&acc)[8]) {
#pragma unroll
  for (int k = 0; k < 4; ++k) {
    const unsigned lo = __builtin_amdgcn_perm(B[k], A[k], 0x05040100u), hi = __builtin_amdgcn_perm(B[k], A[k], 0x07060302u);
    acc[2 * k] = dot2(lo, xp, acc[2 * k]); acc[2 * k + 1] = dot2(hi, xp, acc[2 * k + 1]);
  }
}
__device__ __forceinline__ void pair_perm(u32x4& A, u32x4& B) {
  u32x4 a, b;
  a[0] = __builtin_amdgcn_perm(B[0], A[0], 0x05040100u); a[1] = __builtin_amdgcn_perm(B[0], A[0], 0x07060302u);
  a[2] = __builtin_amdgcn_perm(B[1], A[1], 0x05040100u); a[3] = __builtin_amdgcn_perm(B[1], A[1], 0x07060302u);
  b[0] = __builtin_amdgcn_perm(B[2], A[2], 0x05040100u); b[1] = __builtin_amdgcn_perm(B[2], A[2], 0x07060302u);
  b[2] = __builtin_amdgcn_perm(B[3], A[3], 0x05040100u); b[3] = __builtin_amdgcn_perm(B[3], A[3], 0x07060302u);
  A = a; B = b;
}
#define MKRSRC(ptr, bytes) __builtin_amdgcn_make_buffer_rsrc((void*)(ptr), 0, (int)(bytes), 0x00020000)
#define LDNT(rs, voff, soff) __builtin_amdgcn_raw_buffer_load_b128(rs, voff, soff, 2)
#define LDSC(rs, voff, soff) __builtin_amdgcn_raw_buffer_load_b128(rs, voff, soff, 16)
#define STSC(v, rs, voff) __builtin_amdgcn_raw_buffer_store_b128(v, rs, voff, 0, 16)
#define SB() __builtin_amdgcn_sched_barrier(0)
#define PIN(v) asm volatile("" : "+v"(v))
#define PIN8(a) asm volatile("" : "+v"(a[0]), "+v"(a[1]), "+v"(a[2]), "+v"(a[3]), "+v"(a[4]), "+v"(a[5]), "+v"(a[6]), "+v"(a[7]))
#define TRK(n) do { SB(); __builtin_amdgcn_s_sleep(n); SB(); } while (0)
__device__ __forceinline__ void wg_barrier() {
  asm volatile("s_waitcnt lgkmcnt(0)" ::: "memory");
  __builtin_amdgcn_s_barrier();
  asm volatile("" ::: "memory");
}
__device__ __forceinline__ void lds_arrive(LAS unsigned* c) {
  asm volatile("s_waitcnt lgkmcnt(0)" ::: "memory");
  if ((threadIdx.x & 63) == 0) __hip_atomic_fetch_add(c, 1u, RLX_WG);
}
__device__ __forceinline__ void lds_wait(LAS unsigned* c, unsigned want, int slp) {
#pragma unroll 1
  for (unsigned spins = 0; spins < (1u << 22); ++spins) {
    if (__builtin_amdgcn_readfirstlane(__hip_atomic_load(c, RLX_WG)) >= want) break;
    if (slp) __builtin_amdgcn_s_sleep(16); else __builtin_amdgcn_s_sleep(1);
  }
  asm volatile("" ::: "memory");
}
struct Tag { unsigned lo, hi; };
__device__ __forceinline__ bool gran_ok(unsigned d0, unsigned d1, Tag t) { return (((d0 ^ t.lo) >> 16) | (d1 ^ t.hi)) == 0u; }
__device__ __forceinline__ u32x4 gran_f32(float v, Tag t) { const unsigned u = __float_as_uint(v); return u32x4{t.lo | (u & 0xffffu), t.hi, t.lo | (u >> 16), t.hi}; }
__device__ __forceinline__ bool gran_f32_ok(const u32x4& g, Tag t) { return gran_ok(g[0], g[1], t) && gran_ok(g[2], g[3], t); }
__device__ __forceinline__ float gran_f32_val(const u32x4& g) { return __uint_as_float(__builtin_amdgcn_perm(g[2], g[0], 0x05040100u)); }

__global__ void __launch_bounds__(512, 2) fused_kernel(Params p) {
  __shared__ __attribute__((aligned(16))) float lds[L_END];
  const int tid = threadIdx.x, lane = tid & 63, w = __builtin_amdgcn_readfirstlane(tid >> 6);
  const int j = blockIdx.x & 31, b = blockIdx.x >> 5, h = j & 15, s = j >> 4, half = lane >> 5;
  LAS unsigned* cnt = (LAS unsigned*)(lds + L_CNT);
  STAMP_DECL;
  STAMP(0);
  GAS u64* wsq = (GAS u64*)p.ws;
  const u64 epoch_v = __hip_atomic_load(wsq + WS_EPOCH / 8, RLX_AGENT);
  const __amdgpu_buffer_rsrc_t rws = MKRSRC(p.ws, WS_END);
  u32x4 wo[32];
  const int lane56 = (lane < 56 ? lane : 55) * 16;
  u64 epoch; Tag tag;
#define GET_TAG() { epoch = ((u64)__builtin_amdgcn_readfirstlane((unsigned)(epoch_v >> 32)) << 32) | (unsigned)__builtin_amdgcn_readfirstlane((unsigned)epoch_v); \
    const u64 t48 = ((epoch + 1ull) * 0x9E3779B97F4A7C15ull) >> 16; tag.lo = (unsigned)(t48 << 16); tag.hi = (unsigned)(t48 >> 16); }

  if (w < 4) {
    const size_t base = (size_t)(b * NH + h) * SK * DH;
    const __amdgpu_buffer_rsrc_t rk = MKRSRC(p.kc + base, SK * DH * 2), rv = MKRSRC(p.vc + base, SK * DH * 2);
    const int voff = lane * 16 + w * 2048 + s * 1024;
    u32x4 a[R];
    __builtin_amdgcn_s_barrier();
    SB();
#pragma unroll
    for (int i = 0; i < F; ++i) a[i] = LDNT(rk, voff, i * LS);
    SB();
    __builtin_amdgcn_s_setprio(2);
#pragma unroll
    for (int i = 0; i < P - F; ++i) { PIN(a[i][0]); a[i + F] = LDNT(rk, voff, (i + F) * LS); SB(); }
    STAMP(8);
    GET_TAG();
    lds_wait(cnt + 4, 1u, 0);
    const u32x4 q = *(const u32x4*)(lds + L_Q + (lane & 31) * 4);
    STAMP(1);
    float* pw = lds + L_P + w * 512 + half * 256;
    const int il = ((lane >> 4) & 1) | (((lane >> 3) & 1) << 1) | (((lane >> 2) & 1) << 2) | (((lane >> 1) & 1) << 3) | ((lane & 1) << 4);
    float mx = -INFINITY;
#define KBLOCK(ISSUE, it) { \
      float t1[16], u2[8], u3[4], u4[2], x0 = 0.f; \
      _Pragma("unroll") for (int i = 0; i < 32; ++i) { \
        PIN(a[i][0]); \
        ISSUE; \
        SB(); \
        const u32x4 cur = a[i]; \
        float d = dot2(cur[0], q[0], 0.f); d = dot2(cur[1], q[1], d); d = dot2(cur[2], q[2], d); d = dot2(cur[3], q[3], d); PIN(d); \
        if ((i & 1) == 0) x0 = d; \
        else { t1[i >> 1] = swap16_add(x0, d); \
          if ((i & 3) == 3) { u2[i >> 2] = tstep<XOR8, 8>(t1[(i >> 1) - 1], t1[i >> 1], lane); \
            if ((i & 7) == 7) { u3[i >> 3] = tstep<XOR7, 4>(u2[(i >> 2) - 1], u2[i >> 2], lane); \
              if ((i & 15) == 15) { u4[i >> 4] = tstep<XOR2, 2>(u3[(i >> 3) - 1], u3[i >> 3], lane); \
                if (i == 31) { const float sc = tstep<XOR1, 1>(u4[0], u4[1], lane); mx = fmaxf(mx, sc); pw[(it) * 32 + il] = sc; } } } } } \
        SB(); \
      } }
    KBLOCK(if (i + F >= P) a[(i + F) % R] = LDNT(rk, voff, (i + F) * LS), 0)
#pragma unroll 1
    for (int it = 1; it < 8; ++it) {
      const int so = it * 32 * LS;
      const __amdgpu_buffer_rsrc_t r2 = MKRSRC((it == 7 ? p.vc : p.kc) + base, SK * DH * 2);
      const int so2 = it == 7 ? 0 : so + 32 * LS;
      KBLOCK(if (i + F < 32) a[i + F] = LDNT(rk, voff, so + (i + F) * LS); else a[i + F - 32] = LDNT(r2, voff, so2 + (i + F - 32) * LS), it)
    }
    STAMP(2);
#pragma unroll
    for (int i = F; i < F + VX; ++i) a[i] = LDNT(rv, voff, i * LS);
    SB();
    mx = wave_max(mx);
    float lsum = 0.f;
#pragma unroll
    for (int it = 0; it < 8; ++it) { const float pe = __builtin_amdgcn_exp2f((pw[it * 32 + il] - mx) * C2); lsum += pe; pw[it * 32 + il] = pe; }
    lsum = wave_sum(lsum);
    if (lane == 0) { lds[L_ML + 2 * w] = mx; lds[L_ML + 2 * w + 1] = lsum; }
    lds_arrive(cnt + 0);
    lds_wait(cnt + 2, 4u, 0);
#pragma unroll
    for (int i = 0; i < 32; ++i) wo[i] = *(const u32x4*)((const unsigned char*)(lds + L_WS) + (w * 32 + i) * 896 + lane56);
    float acc[8];
#pragma unroll
    for (int k = 0; k < 8; ++k) acc[k] = 0.f;
    f32x4 pv, pn = *(const f32x4*)pw;
#pragma unroll 1
    for (int it = 0; it < 8; ++it) {
      const int so = it * 32 * LS;
      const int voff2 = it == 7 ? OOB : voff;
#pragma unroll
      for (int i = 0; i < 32; ++i) {
        if ((i & 3) == 0) { pv = pn; pn = *(const f32x4*)(pw + it * 32 + i + 4); }
        PIN(a[i][0]);
        if (i >= VX || it > 0) a[(i + F) % R] = LDNT(rv, i + F < 32 ? voff : voff2, so + (i + F) * LS);
        SB();
        const u32x4 cur = a[i]; const float pe = pv[i & 3];
#pragma unroll
        for (int k = 0; k < 4; ++k) { acc[2 * k] = fmaf(pe, bflo(cur[k]), acc[2 * k]); acc[2 * k + 1] = fmaf(pe, bfhi(cur[k]), acc[2 * k + 1]); }
        PIN8(acc);
        SB();
      }
    }
    STAMP(3);
    f32x4 r;
#pragma unroll
    for (int k = 0; k < 4; ++k) r[k] = swap32_add(acc[k], acc[k + 4]);
    *(f32x4*)(lds + L_OP + w * 256 + (lane & 31) * 8 + half * 4) = r;
  } else {
    const int hw = w - 4;
    if (tid == 256) { cnt[0] = 0u; cnt[1] = 0u; cnt[2] = 0u; cnt[3] = 0u; cnt[4] = 0u; }
    {
      const int c = 2 * b + s;
      const __amdgpu_buffer_rsrc_t rq = MKRSRC(p.wq + h * DH + 16 * c, (size_t)DM * NH * DH * 2 - (h * DH + 16 * c) * 2);
      const __amdgpu_buffer_rsrc_t rx = MKRSRC(p.x, NB * DM * 2);
      const int wv = (lane >> 1) * 8192 + (lane & 1) * 16;
      const int xvo = (lane & 8) ? OOB : (lane & 7) * (DM * 2) + (lane >> 4) * 16;
      u32x4 wt[4], xt[4];
#pragma unroll
      for (int i = 0; i < 4; ++i) {
        const int t = hw + 4 * i;
        wt[i] = LDNT(rq, t < 14 ? wv : OOB, t < 14 ? t * 32 * 8192 : 0);
        xt[i] = __builtin_amdgcn_raw_buffer_load_b128(rx, t < 14 ? xvo : OOB, t < 14 ? t * 64 : 0, 0);
      }
      SB();
      wg_barrier();
      SB();
      float* qs = lds + L_QS + hw * 1024;
#pragma unroll
      for (int i = 0; i < 4; ++i) *(u32x4*)(qs + i * 256 + lane * 4) = wt[i];
      asm volatile("s_waitcnt lgkmcnt(0)" ::: "memory");
      f32x4 d = {0.f, 0.f, 0.f, 0.f};
      const int tro = (8 * (lane >> 4) + ((lane & 15) >> 2)) * 32 + (lane & 3) * 8;
#pragma unroll
      for (int i = 0; i < 4; ++i) {
        LAS unsigned char* tb = (LAS unsigned char*)(qs + i * 256) + tro;
        const s16x4 r0 = __builtin_amdgcn_ds_read_tr16_b64_v4i16((LAS s16x4*)tb);
        const s16x4 r1 = __builtin_amdgcn_ds_read_tr16_b64_v4i16((LAS s16x4*)(tb + 128));
        const s16x8 bb = __builtin_shufflevector(r0, r1, 0, 1, 2, 3, 4, 5, 6, 7);
        d = __builtin_amdgcn_mfma_f32_16x16x32_bf16(__builtin_bit_cast(bf16x8, xt[i]), __builtin_bit_cast(bf16x8, bb), d, 0, 0, 0);
      }
      if (lane < 32) *(f32x4*)(lds + L_QR + hw * 128 + lane * 4) = d;
      lds_arrive(cnt + 3);
      GET_TAG();
      STAMP(8);
      if (hw < 2) {
        lds_wait(cnt + 3, 4u, 0);
        const int t = tid - 256, bb = t >> 4, n = t & 15;
        const float* qr = lds + L_QR + ((bb >> 2) * 16 + n) * 4 + (bb & 3);
        const float qv = (qr[0] + qr[128]) + (qr[256] + qr[384]);
        const u64 g = ((u64)tag.hi << 32) | (tag.lo | (cvtpk(qv, 0.f) & 0xffffu));
        __hip_atomic_store(wsq + WS_Q / 8 + (h * 8 + bb) * 256 + 16 * c + n, g, RLX_AGENT);
      }
      if (hw == 0) {
        __builtin_amdgcn_s_sleep(QDELAY);
        const int qo = WS_Q + ((h * 8 + b) * 256 + 4 * lane) * 8;
        u32x4 g0, g1;
#pragma unroll 1
        for (unsigned spins = 0; spins < SPIN_CAP; ++spins) {
          asm volatile("" ::: "memory");
          g0 = LDSC(rws, qo, 0); g1 = LDSC(rws, qo, 16);
          const bool ok = gran_ok(g0[0], g0[1], tag) && gran_ok(g0[2], g0[3], tag) && gran_ok(g1[0], g1[1], tag) && gran_ok(g1[2], g1[3], tag);
          if (__all(ok)) break;
        }
        *(u32x2*)(lds + L_Q + lane * 2) = u32x2{__builtin_amdgcn_perm(g0[2], g0[0], 0x05040100u), __builtin_amdgcn_perm(g1[2], g1[0], 0x05040100u)};
        lds_arrive(cnt + 4);
      }
    }
    STAMP(1);
    if (j < 28) {
      const int ch = lane & 7, rg = lane >> 3;
      const __amdgpu_buffer_rsrc_t rg_ = MKRSRC(p.wg + 64 * j, (size_t)DM * FF * 2 - 128 * j), ru_ = MKRSRC(p.wu + 64 * j, (size_t)DM * FF * 2 - 128 * j);
      const int voff = (112 * hw + 2 * rg) * (FF * 2) + ch * 16;
      u32x4 g[14], u[14]; unsigned xd[7];
      const unsigned* xp = (const unsigned*)(p.x + b * DM) + 56 * hw + rg;
#pragma unroll
      for (int i = 0; i < 7; ++i) xd[i] = xp[8 * i];
#pragma unroll
      for (int i = 0; i < 7; ++i) {
        g[2 * i] = LDNT(rg_, voff, (16 * i) * (FF * 2)); TRK(HSLEEP); g[2 * i + 1] = LDNT(rg_, voff, (16 * i + 1) * (FF * 2)); TRK(HSLEEP);
        u[2 * i] = LDNT(ru_, voff, (16 * i) * (FF * 2)); TRK(HSLEEP); u[2 * i + 1] = LDNT(ru_, voff, (16 * i + 1) * (FF * 2)); TRK(HSLEEP); }
      const __amdgpu_buffer_rsrc_t rd = MKRSRC(p.wd + (size_t)64 * j * DM, (size_t)(FF - 64 * j) * DM * 2);
      u32x4 dn[16];
#pragma unroll
      for (int i = 0; i < 16; ++i) { dn[i] = LDNT(rd, lane56, (16 * hw + i) * (DM * 2)); TRK(HSLEEP); }
      SB();
      float v[16];
      { float ag[8], au[8];
#pragma unroll
        for (int k = 0; k < 8; ++k) { ag[k] = 0.f; au[k] = 0.f; }
#pragma unroll
        for (int i = 0; i < 7; ++i) { pair_mac(g[2 * i], g[2 * i + 1], xd[i], ag); pair_mac(u[2 * i], u[2 * i + 1], xd[i], au); }
#pragma unroll
        for (int k = 0; k < 8; ++k) { v[k] = ag[k]; v[k + 8] = au[k]; } }
      float A[8], Bq[4]; f32x2 Cq;
#pragma unroll
      for (int k = 0; k < 8; ++k) A[k] = swap32_add(v[k], v[k + 8]);
#pragma unroll
      for (int k = 0; k < 4; ++k) Bq[k] = swap16_add(A[k], A[k + 4]);
      Cq[0] = tstep<XOR8, 8>(Bq[0], Bq[2], lane); Cq[1] = tstep<XOR8, 8>(Bq[1], Bq[3], lane);
      *(f32x2*)(lds + L_F1 + hw * 128 + half * 64 + 8 * ch + 2 * ((lane >> 3) & 1) + 4 * ((lane >> 4) & 1)) = Cq;
      lds_arrive(cnt + 1);
      lds_wait(cnt + 1, 4u, 0);
      const int col = 16 * hw + (lane & 15);
      float gs = 0.f, us = 0.f;
#pragma unroll
      for (int i = 0; i < 4; ++i) { gs += lds[L_F1 + i * 128 + col]; us += lds[L_F1 + i * 128 + 64 + col]; }
      const float hv = gs / (1.f + __expf(-gs)) * us;
      const unsigned hp = cvtpk(hv, dpp<XOR1>(hv));
      float fa[8];
#pragma unroll
      for (int k = 0; k < 8; ++k) fa[k] = 0.f;
#pragma unroll
      for (int i = 0; i < 8; ++i) pair_mac(dn[2 * i], dn[2 * i + 1], __builtin_amdgcn_readlane(hp, 2 * i), fa);
      if (lane < 56) { float* fp = lds + L_FP + hw * 448 + lane * 8; *(f32x4*)fp = f32x4{fa[0], fa[1], fa[2], fa[3]}; *(f32x4*)(fp + 4) = f32x4{fa[4], fa[5], fa[6], fa[7]}; }
    } else {
      if (lane < 56) { float* fp = lds + L_FP + hw * 448 + lane * 8; *(f32x4*)fp = f32x4{0.f, 0.f, 0.f, 0.f}; *(f32x4*)(fp + 4) = f32x4{0.f, 0.f, 0.f, 0.f}; }
    }
    STAMP(2);
    const __amdgpu_buffer_rsrc_t rwo = MKRSRC(p.wo + (size_t)h * DH * DM, (size_t)(NH - h) * DH * DM * 2);
#pragma unroll
    for (int i = 0; i < 32; ++i) { wo[i] = LDNT(rwo, lane56, (32 * hw + i) * (DM * 2)); TRK(HSLEEP); }
    SB();
#pragma unroll
    for (int i = 0; i < 16; ++i) {
      pair_perm(wo[2 * i], wo[2 * i + 1]);
      if (lane < 56) { unsigned char* dst = (unsigned char*)(lds + L_WS) + (hw * 32 + 2 * i) * 896 + lane * 16; *(u32x4*)dst = wo[2 * i]; *(u32x4*)(dst + 896) = wo[2 * i + 1]; }
    }
    lds_arrive(cnt + 2);
    STAMP(9);
    if (hw == 0) {
      float gp = 0.f;
#pragma unroll
      for (int i = 0; i < 7; ++i) gp += __uint_as_float((unsigned)p.x[b * DM + lane + 64 * i] << 16) * __uint_as_float((unsigned)p.gw[lane + 64 * i] << 16);
      gp = wave_sum(gp);
      const float gate = 1.f / (1.f + __expf(-gp));
      lds_wait(cnt + 0, 4u, 1);
      const float mw = lds[L_ML + 2 * (lane & 3)], lw = lds[L_ML + 2 * (lane & 3) + 1];
      float Ms = fmaxf(mw, dpp<XOR1>(mw)); Ms = fmaxf(Ms, dpp<XOR2>(Ms));
      float Ls = lw * __builtin_amdgcn_exp2f((mw - Ms) * C2); Ls += dpp<XOR1>(Ls); Ls += dpp<XOR2>(Ls);
      if (lane < 2) STSC(gran_f32(lane ? Ls : Ms, tag), rws, WS_ML + blockIdx.x * 128 + lane * 16);
      u32x4 gq;
#pragma unroll 1
      for (unsigned spins = 0; spins < SPIN_CAP; ++spins) {
        asm volatile("" ::: "memory");
        gq = LDSC(rws, WS_ML + (blockIdx.x ^ 16) * 128 + (lane & 1) * 16, 0);
        if (__all(gran_f32_ok(gq, tag))) break;
        __builtin_amdgcn_s_sleep(8);
      }
      const float gv = gran_f32_val(gq), go = dpp<XOR1>(gv);
      const float Mp = (lane & 1) ? go : gv, Lp = (lane & 1) ? gv : go;
      const float M = fmaxf(Ms, Mp);
      const float L = Ls * __builtin_amdgcn_exp2f((Ms - M) * C2) + Lp * __builtin_amdgcn_exp2f((Mp - M) * C2);
      if (lane < 4) lds[L_FAC + lane] = gate * __builtin_amdgcn_exp2f((mw - M) * C2) / L;
      if (lane == 0) lds[L_FAC + 4] = 1.f - gate;
    } else {
      lds_wait(cnt + 0, 4u, 1);
    }
#pragma unroll
    for (int i = 0; i < 32; ++i) { wo[i] = LDNT(rwo, lane56, (32 * w + i) * (DM * 2)); TRK(HSLEEP2); }
    SB();
#pragma unroll
    for (int i = 0; i < 16; ++i) pair_perm(wo[2 * i], wo[2 * i + 1]);
    STAMP(3);
  }
  const int zst = WS_Z + (((b * 32 + tid / 14) * 32 + j) * 16 + tid % 14) * 16;
  const int zld = WS_Z + (b * 32 + j) * 8192 + tid * 16;
  wg_barrier();
  STAMP(4);
  {
    const int wp = (lane >> 3) & 3;
    f32x4 o4 = *(const f32x4*)(lds + L_OP + wp * 256 + 32 * w + 4 * (lane & 7));
    const float fc = lds[L_FAC + wp];
#pragma unroll
    for (int k = 0; k < 4; ++k) { float t = o4[k] * fc; t += dpp<XOR8>(t); o4[k] = swap16_add(t, t); }
    const unsigned d0 = cvtpk(o4[0], o4[1]), d1 = cvtpk(o4[2], o4[3]);
    float y[8];
#pragma unroll
    for (int k = 0; k < 8; ++k) y[k] = 0.f;
#pragma unroll
    for (int i = 0; i < 16; ++i) {
      const unsigned op = __builtin_amdgcn_readlane((i & 1) ? d1 : d0, i >> 1);
#pragma unroll
      for (int k = 0; k < 4; ++k) { y[k] = dot2(wo[2 * i][k], op, y[k]); y[4 + k] = dot2(wo[2 * i + 1][k], op, y[4 + k]); }
    }
    if (lane < 56) { float* yp = lds + L_YP + w * 448 + lane * 8; *(f32x4*)yp = f32x4{y[0], y[1], y[2], y[3]}; *(f32x4*)(yp + 4) = f32x4{y[4], y[5], y[6], y[7]}; }
  }
  wg_barrier();
  if (tid < DM) {
    float z = 0.f, f = 0.f;
#pragma unroll
    for (int i = 0; i < 8; ++i) z += lds[L_YP + i * 448 + tid];
#pragma unroll
    for (int i = 0; i < 4; ++i) f += lds[L_FP + i * 448 + tid];
    z = fmaf(lds[L_FAC + 4], f, z);
    STSC(gran_f32(z, tag), rws, zst);
  }
  STAMP(5);
  {
    float zv = 0.f;
    bool done = (tid & 15) >= 14;
#pragma unroll 1
    for (unsigned spins = 0; spins < SPIN_CAP; ++spins) {
      asm volatile("" ::: "memory");
      if (!done) { const u32x4 g = LDSC(rws, zld, 0); if (gran_f32_ok(g, tag)) { zv = gran_f32_val(g); done = true; } }
      if (__all(done)) break;
      __builtin_amdgcn_s_sleep(2);
    }
    zv = swap16_add(zv, zv); zv = swap32_add(zv, zv);
    if (lane < 16) lds[L_FIN + w * 16 + lane] = zv;
  }
  STAMP(6);
  wg_barrier();
  if (tid < 14) {
    float o = 0.f;
#pragma unroll
    for (int i = 0; i < 8; ++i) o += lds[L_FIN + i * 16 + tid];
    p.out[b * DM + 14 * j + tid] = (u16)(cvtpk(o, 0.f) & 0xffffu);
  }
  if (blockIdx.x == 0 && tid == 64) __hip_atomic_store(wsq + WS_EPOCH / 8, epoch + 1ull, RLX_AGENT);
  STAMP(7);
  STAMP_FLUSH;
}

extern "C" void kernel_launch(void* const* d_in, const int* in_sizes, int n_in, void* d_out, int out_size, void* d_ws, size_t ws_size, hipStream_t stream) {
  Params p;
  p.x = (const u16*)d_in[0]; p.kc = (const u16*)d_in[1]; p.vc = (const u16*)d_in[2]; p.wq = (const u16*)d_in[3]; p.wo = (const u16*)d_in[4];
  p.wg = (const u16*)d_in[6]; p.wu = (const u16*)d_in[7]; p.wd = (const u16*)d_in[8]; p.gw = (const u16*)d_in[9];
  p.out = (u16*)d_out; p.ws = (unsigned char*)d_ws;
  fused_kernel<<<256, 512, 0, stream>>>(p);
}
```

```cpp
#include <hip/hip_runtime.h>
#include <hip/hip_bf16.h>
#include <stdint.h>

typedef unsigned u32x4 __attribute__((ext_vector_type(4)));
typedef unsigned u32x2 __attribute__((ext_vector_type(2)));
typedef float f32x4 __attribute__((ext_vector_type(4)));
typedef float f32x2 __attribute__((ext_vector_type(2)));
typedef __bf16 bf16x2 __attribute__((ext_vector_type(2)));
typedef unsigned short u16;
typedef unsigned long long u64;
#define GAS __attribute__((address_space(1)))
#define LAS __attribute__((address_space(3)))
#define RLX_AGENT __ATOMIC_RELAXED, __HIP_MEMORY_SCOPE_AGENT
#define RLX_WG __ATOMIC_RELAXED, __HIP_MEMORY_SCOPE_WORKGROUP

constexpr int NB = 8, NH = 16, SK = 4096, DH = 256, DM = 448, FF = 1792;
#define KF 5
#define KP 20
#define WAUX 2
#define KEARLY 2
#define HSLEEP 12
constexpr int F = KF, P = KP, R = 32;
constexpr int NL = 256;
constexpr int LS = 8192;
constexpr float C2 = 0.0625f * 1.4426950408889634f;
constexpr unsigned SPIN_CAP = 1u << 18;

constexpr size_t WS_EPOCH = 0;
constexpr size_t WS_FLAG = 4096;
constexpr size_t WS_ML = 8192;
constexpr size_t WS_Z = WS_ML + 8 * 32 * 128;
constexpr size_t WS_END = WS_Z + (size_t)8 * 32 * 32 * 128;
#define STAMP(k) do {} while (0)
#define STAMP_DECL do {} while (0)
#define STAMP_FLUSH do {} while (0)

constexpr int L_QP = 0;
constexpr int L_P = L_QP + 2048;
constexpr int L_OP = L_P + 2048;
constexpr int L_YP = L_OP + 1024;
constexpr int L_FP = L_YP + 8 * 448;
constexpr int L_F1 = L_FP + 4 * 448;
constexpr int L_FIN = L_F1 + 512;
constexpr int L_ML = L_FIN + 128;
constexpr int L_FAC = L_ML + 8;
constexpr int L_CNT = L_FAC + 8;
constexpr int L_END = L_CNT + 8;

struct Params {
  const u16 *x, *kc, *vc, *wq, *wo, *wg, *wu, *wd, *gw;
  u16* out;
  unsigned char* ws;
};

__device__ __forceinline__ float bflo(unsigned v) { return __uint_as_float(v << 16); }
__device__ __forceinline__ float bfhi(unsigned v) { return __uint_as_float(v & 0xffff0000u); }
__device__ __forceinline__ unsigned cvtpk(float lo, float hi) {
  unsigned r; asm("v_cvt_pk_bf16_f32 %0, %1, %2" : "=v"(r) : "v"(lo), "v"(hi)); return r;
}
__device__ __forceinline__ float dot2(unsigned k, unsigned q, float acc) {
  return __builtin_amdgcn_fdot2_f32_bf16(__builtin_bit_cast(bf16x2, k), __builtin_bit_cast(bf16x2, q), acc, false);
}
template <int CTRL> __device__ __forceinline__ float dpp(float x) {
  return __builtin_bit_cast(float, __builtin_amdgcn_mov_dpp(__builtin_bit_cast(int, x), CTRL, 0xf, 0xf, true));
}
constexpr int XOR1 = 0xB1, XOR2 = 0x4E, XOR7 = 0x141, XOR8 = 0x128;
__device__ __forceinline__ float swap16_add(float a, float b) {
  auto s = __builtin_amdgcn_permlane16_swap(__float_as_uint(a), __float_as_uint(b), false, false);
  return __uint_as_float(s[0]) + __uint_as_float(s[1]);
}
__device__ __forceinline__ float swap32_add(float a, float b) {
  auto s = __builtin_amdgcn_permlane32_swap(__float_as_uint(a), __float_as_uint(b), false, false);
  return __uint_as_float(s[0]) + __uint_as_float(s[1]);
}
template <int CTRL, int BIT> __device__ __forceinline__ float tstep(float lo, float hi, int lane) {
  const float t1 = lo + dpp<CTRL>(lo), t2 = hi + dpp<CTRL>(hi);
  return (lane & BIT) ? t2 : t1;
}
__device__ __forceinline__ float wave_max(float x) {
  x = fmaxf(x, dpp<XOR1>(x)); x = fmaxf(x, dpp<XOR2>(x)); x = fmaxf(x, dpp<XOR7>(x)); x = fmaxf(x, dpp<XOR8>(x));
  auto s = __builtin_amdgcn_permlane16_swap(__float_as_uint(x), __float_as_uint(x), false, false);
  x = fmaxf(__uint_as_float(s[0]), __uint_as_float(s[1]));
  auto t = __builtin_amdgcn_permlane32_swap(__float_as_uint(x), __float_as_uint(x), false, false);
  return fmaxf(__uint_as_float(t[0]), __uint_as_float(t[1]));
}
__device__ __forceinline__ float wave_sum(float x) {
  x = x + dpp<XOR1>(x); x = x + dpp<XOR2>(x); x = x + dpp<XOR7>(x); x = x + dpp<XOR8>(x);
  auto s = __builtin_amdgcn_permlane16_swap(__float_as_uint(x), __float_as_uint(x), false, false);
  x = __uint_as_float(s[0]) + __uint_as_float(s[1]);
  auto t = __builtin_amdgcn_permlane32_swap(__float_as_uint(x), __float_as_uint(x), false, false);
  return __uint_as_float(t[0]) + __uint_as_float(t[1]);
}
__device__ __forceinline__ void pair_mac(const u32x4& A, const u32x4& B, unsigned xp, float (&acc)[8]) {
#pragma unroll
  for (int k = 0; k < 4; ++k) {
    const unsigned lo = __builtin_amdgcn_perm(B[k], A[k], 0x05040100u), hi = __builtin_amdgcn_perm(B[k], A[k], 0x07060302u);
    acc[2 * k] = dot2(lo, xp, acc[2 * k]); acc[2 * k + 1] = dot2(hi, xp, acc[2 * k + 1]);
  }
}
__device__ __forceinline__ void pair_perm(u32x4& A, u32x4& B) {
  u32x4 a, b;
  a[0] = __builtin_amdgcn_perm(B[0], A[0], 0x05040100u); a[1] = __builtin_amdgcn_perm(B[0], A[0], 0x07060302u);
  a[2] = __builtin_amdgcn_perm(B[1], A[1], 0x05040100u); a[3] = __builtin_amdgcn_perm(B[1], A[1], 0x07060302u);
  b[0] = __builtin_amdgcn_perm(B[2], A[2], 0x05040100u); b[1] = __builtin_amdgcn_perm(B[2], A[2], 0x07060302u);
  b[2] = __builtin_amdgcn_perm(B[3], A[3], 0x05040100u); b[3] = __builtin_amdgcn_perm(B[3], A[3], 0x07060302u);
  A = a; B = b;
}
#define MKRSRC(ptr, bytes) __builtin_amdgcn_make_buffer_rsrc((void*)(ptr), 0, (int)(bytes), 0x00020000)
#define LDNT(rs, voff, soff) __builtin_amdgcn_raw_buffer_load_b128(rs, voff, soff, 2)
#define LDW(rs, voff, soff) __builtin_amdgcn_raw_buffer_load_b128(rs, voff, soff, WAUX)
#define SB() __builtin_amdgcn_sched_barrier(0)
#define PIN(v) asm volatile("" : "+v"(v))
#define TRK() do { SB(); __builtin_amdgcn_s_sleep(HSLEEP); SB(); } while (0)
__device__ __forceinline__ void wg_barrier() {
  asm volatile("s_waitcnt lgkmcnt(0)" ::: "memory");
  __builtin_amdgcn_s_barrier();
  asm volatile("" ::: "memory");
}
__device__ __forceinline__ void lds_arrive(LAS unsigned* c) {
  asm volatile("s_waitcnt lgkmcnt(0)" ::: "memory");
  if ((threadIdx.x & 63) == 0) __hip_atomic_fetch_add(c, 1u, RLX_WG);
}
__device__ __forceinline__ void lds_wait(LAS unsigned* c, unsigned want, int slp) {
#pragma unroll 1
  for (unsigned spins = 0; spins < (1u << 24); ++spins) {
    if (__builtin_amdgcn_readfirstlane(__hip_atomic_load(c, RLX_WG)) >= want) break;
    if (slp) __builtin_amdgcn_s_sleep(16); else __builtin_amdgcn_s_sleep(1);
  }
  asm volatile("" ::: "memory");
}
__device__ __forceinline__ u64 granule(float v) { return (1ull << 32) | (u64)__float_as_uint(v); }

__global__ void __launch_bounds__(512, 2) fused_kernel(Params p) {
  __shared__ __attribute__((aligned(16))) float lds[L_END];
  const int tid = threadIdx.x, lane = tid & 63, w = __builtin_amdgcn_readfirstlane(tid >> 6);
  const int j = blockIdx.x & 31, b = blockIdx.x >> 5, h = j & 15, s = j >> 4;
  LAS unsigned* cnt = (LAS unsigned*)(lds + L_CNT);
  STAMP_DECL;
  STAMP(0);
  u32x4 wo[32];
  const __amdgpu_buffer_rsrc_t rwo = MKRSRC(p.wo + (size_t)h * DH * DM, (size_t)(NH - h) * DH * DM * 2);
  const int lane56 = (lane < 56 ? lane : 55) * 16;

  const int half = lane >> 5;
  const __amdgpu_buffer_rsrc_t rq = MKRSRC(p.wq + h * DH, (size_t)DM * NH * DH * 2 - h * DH * 2);
  const int qoff = (56 * w + 28 * half) * 8192 + (lane & 31) * 16;
  typedef unsigned u32x4a8 __attribute__((ext_vector_type(4), aligned(8)));
  const unsigned char* xb = (const unsigned char*)p.x + b * (DM * 2) + w * 112 + half * 56;
#define QX_LOADS() u32x4 xv[4]; xv[0] = *(const u32x4a8*)xb; xv[1] = *(const u32x4a8*)(xb + 16); xv[2] = *(const u32x4a8*)(xb + 32); \
    { const u32x2 t_ = *(const u32x2*)(xb + 48); xv[3] = u32x4{t_[0], t_[1], 0u, 0u}; }
#define Q_COMPUTE() { float acc[8]; \
    _Pragma("unroll") for (int k = 0; k < 8; ++k) acc[k] = 0.f; \
    _Pragma("unroll") for (int n = 0; n < 28; n += 2) { pair_mac(qa[n], qa[n + 1], xv[n >> 3][(n >> 1) & 3], acc); \
      asm volatile("" : "+v"(acc[0]), "+v"(acc[1]), "+v"(acc[2]), "+v"(acc[3]), "+v"(acc[4]), "+v"(acc[5]), "+v"(acc[6]), "+v"(acc[7])); \
      if (n == 0) STAMP(8); \
      SB(); } \
    f32x4 r; \
    _Pragma("unroll") for (int k = 0; k < 4; ++k) r[k] = swap32_add(acc[k], acc[k + 4]); \
    *(f32x4*)(lds + L_QP + w * 256 + (lane & 31) * 8 + half * 4) = r; STAMP(9); }

  if (w < 4) {
    const size_t base = (size_t)(b * NH + h) * SK * DH;
    const __amdgpu_buffer_rsrc_t rk = MKRSRC(p.kc + base, SK * DH * 2), rv = MKRSRC(p.vc + base, SK * DH * 2);
    const int voff = lane * 16 + w * 2048 + s * 1024;
    u32x4 a[R];
    {
#pragma unroll
      for (int i = 0; i < KEARLY; ++i) a[i] = LDNT(rk, voff, i * LS);
      QX_LOADS();
      u32x4 qa[28];
#pragma unroll
      for (int i = 0; i < 28; ++i) qa[i] = LDW(rq, qoff, i * 8192);
      SB();
      __builtin_amdgcn_s_barrier();
      SB();
#pragma unroll
      for (int i = KEARLY; i < P; ++i) a[i] = LDNT(rk, voff, i * LS);
      SB();
      __builtin_amdgcn_s_setprio(2);
      Q_COMPUTE();
    }
    wg_barrier();
    STAMP(1);
    u32x4 q;
    {
      const float* qp = lds + L_QP + (lane & 31) * 8 + (lane >> 5) * 4;
      f32x4 t = *(const f32x4*)qp;
#pragma unroll
      for (int i = 1; i < 8; ++i) t += *(const f32x4*)(qp + i * 256);
      const unsigned d0 = cvtpk(t[0], t[1]), d1 = cvtpk(t[2], t[3]);
      auto s0 = __builtin_amdgcn_permlane32_swap(d0, d0, false, false);
      auto s1 = __builtin_amdgcn_permlane32_swap(d1, d1, false, false);
      q[0] = s0[0]; q[1] = s1[0]; q[2] = s0[1]; q[3] = s1[1];
    }
    float* pw = lds + L_P + w * 512 + (lane >> 5) * 256;
    const int il = ((lane >> 4) & 1) | (((lane >> 3) & 1) << 1) | (((lane >> 2) & 1) << 2) | (((lane >> 1) & 1) << 3) | ((lane & 1) << 4);
    float mx = -INFINITY;
#define KBLOCK(MODE, so, it) { \
      float t1[16], u2[8], u3[4], u4[2], x0 = 0.f; \
      _Pragma("unroll") for (int i = 0; i < 32; ++i) { \
        PIN(a[i % R][0]); \
        if ((MODE) != 0 || i + F >= P) { \
          if ((MODE) == 2 && i + F >= 32) a[(i + F) % R] = LDNT(rv, voff, (i + F - 32) * LS); \
          else a[(i + F) % R] = LDNT(rk, voff, (so) + (i + F) * LS); } \
        SB(); \
        const u32x4 cur = a[i % R]; \
        float d = dot2(cur[0], q[0], 0.f); d = dot2(cur[1], q[1], d); d = dot2(cur[2], q[2], d); d = dot2(cur[3], q[3], d); PIN(d); \
        if ((i & 1) == 0) x0 = d; \
        else { t1[i >> 1] = swap16_add(x0, d); \
          if ((i & 3) == 3) { u2[i >> 2] = tstep<XOR8, 8>(t1[(i >> 1) - 1], t1[i >> 1], lane); \
            if ((i & 7) == 7) { u3[i >> 3] = tstep<XOR7, 4>(u2[(i >> 2) - 1], u2[i >> 2], lane); \
              if ((i & 15) == 15) { u4[i >> 4] = tstep<XOR2, 2>(u3[(i >> 3) - 1], u3[i >> 3], lane); \
                if (i == 31) { const float sc = tstep<XOR1, 1>(u4[0], u4[1], lane); mx = fmaxf(mx, sc); pw[(it) * 32 + il] = sc; } } } } } \
        SB(); \
      } }
    KBLOCK(0, 0, 0)
    for (int it = 1; it < 7; ++it) KBLOCK(1, it * 32 * LS, it)
    KBLOCK(2, 7 * 32 * LS, 7)
    STAMP(2);
    mx = wave_max(mx);
    float lsum = 0.f;
#pragma unroll
    for (int it = 0; it < 8; ++it) { const float pe = __builtin_amdgcn_exp2f((pw[it * 32 + il] - mx) * C2); lsum += pe; pw[it * 32 + il] = pe; }
    lsum = wave_sum(lsum);
    if (lane == 0) { lds[L_ML + 2 * w] = mx; lds[L_ML + 2 * w + 1] = lsum; }
    lds_arrive(cnt + 0);
    float acc[8];
#pragma unroll
    for (int k = 0; k < 8; ++k) acc[k] = 0.f;
    f32x4 pv;
#define VBLOCK(MODE, so, it) { \
      _Pragma("unroll") for (int i = 0; i < 32; ++i) { \
        if ((i & 3) == 0) pv = *(const f32x4*)(pw + (it) * 32 + i); \
        PIN(a[i % R][0]); \
        if ((MODE) != 2 || i + F < 32) a[(i + F) % R] = LDNT(rv, voff, (so) + (i + F) * LS); \
        if ((MODE) == 0) wo[i] = LDW(rwo, lane56, (32 * w + i) * (DM * 2)); \
        SB(); \
        const u32x4 cur = a[i % R]; const float pe = pv[i & 3]; \
        _Pragma("unroll") for (int k = 0; k < 4; ++k) { acc[2 * k] = fmaf(pe, bflo(cur[k]), acc[2 * k]); acc[2 * k + 1] = fmaf(pe, bfhi(cur[k]), acc[2 * k + 1]); } \
        asm volatile("" : "+v"(acc[0]), "+v"(acc[1]), "+v"(acc[2]), "+v"(acc[3]), "+v"(acc[4]), "+v"(acc[5]), "+v"(acc[6]), "+v"(acc[7])); \
        if ((MODE) == 2 && i < 16) { pair_perm(wo[2 * i], wo[2 * i + 1]); asm volatile("" : "+v"(wo[2 * i]), "+v"(wo[2 * i + 1])); } \
        SB(); \
      } }
    VBLOCK(0, 0, 0)
    for (int it = 1; it < 7; ++it) VBLOCK(1, it * 32 * LS, it)
    VBLOCK(2, 7 * 32 * LS, 7)
    STAMP(3);
    f32x4 r;
#pragma unroll
    for (int k = 0; k < 4; ++k) r[k] = swap32_add(acc[k], acc[k + 4]);
    *(f32x4*)(lds + L_OP + w * 256 + (lane & 31) * 8 + (lane >> 5) * 4) = r;
  } else {
    const int hw = w - 4;
    if (tid == 256) { cnt[0] = 0u; cnt[1] = 0u; }
    GAS u64* wsq = (GAS u64*)p.ws;
    u64 epoch = 0;
    {
      QX_LOADS();
      u32x4 qa[28];
#pragma unroll
      for (int i = 0; i < 28; ++i) qa[i] = LDW(rq, qoff, i * 8192);
      SB();
      __builtin_amdgcn_s_barrier();
      SB();
      Q_COMPUTE();
    }
    wg_barrier();
    STAMP(1);
    if (hw == 0) {
      epoch = __hip_atomic_load(wsq + (WS_EPOCH / 8) + b * 32, RLX_AGENT);
      const __amdgpu_buffer_rsrc_t rz = MKRSRC(p.ws, WS_END);
      const u32x4 z4 = {0u, 0u, 0u, 0u};
#pragma unroll
      for (int i = 0; i < 4; ++i) {
        const int c = 8 * i + (lane >> 3);
        __builtin_amdgcn_raw_buffer_store_b128(z4, rz, (int)WS_Z + ((b * 32 + c) * 32 + j) * 128 + (lane & 7) * 16, 0, 16);
      }
      if (lane == 0) __builtin_amdgcn_raw_buffer_store_b128(z4, rz, (int)WS_ML + (b * 32 + j) * 128, 0, 16);
      asm volatile("s_waitcnt vmcnt(0)" ::: "memory");
      if (lane == 0) __hip_atomic_store(wsq + (WS_FLAG / 8) + b * 32 + j, epoch + 1, RLX_AGENT);
    }
    if (j < 28) {
      const int ch = lane & 7, rg = lane >> 3;
      const __amdgpu_buffer_rsrc_t rg_ = MKRSRC(p.wg + 64 * j, (size_t)DM * FF * 2 - 128 * j), ru_ = MKRSRC(p.wu + 64 * j, (size_t)DM * FF * 2 - 128 * j);
      const int voff = (112 * hw + 2 * rg) * (FF * 2) + ch * 16;
      u32x4 g[14], u[14]; unsigned xd[7];
      const unsigned* xp = (const unsigned*)(p.x + b * DM) + 56 * hw + rg;
#pragma unroll
      for (int i = 0; i < 7; ++i) xd[i] = xp[8 * i];
#pragma unroll
      for (int i = 0; i < 7; ++i) {
        g[2 * i] = LDW(rg_, voff, (16 * i) * (FF * 2)); TRK(); g[2 * i + 1] = LDW(rg_, voff, (16 * i + 1) * (FF * 2)); TRK();
        u[2 * i] = LDW(ru_, voff, (16 * i) * (FF * 2)); TRK(); u[2 * i + 1] = LDW(ru_, voff, (16 * i + 1) * (FF * 2)); TRK(); }
      const __amdgpu_buffer_rsrc_t rd = MKRSRC(p.wd + (size_t)64 * j * DM, (size_t)(FF - 64 * j) * DM * 2);
      u32x4 dn[16];
#pragma unroll
      for (int i = 0; i < 16; ++i) { dn[i] = LDW(rd, lane56, (16 * hw + i) * (DM * 2)); TRK(); }
      SB();
      float v[16];
      { float ag[8], au[8];
#pragma unroll
        for (int k = 0; k < 8; ++k) { ag[k] = 0.f; au[k] = 0.f; }
#pragma unroll
        for (int i = 0; i < 7; ++i) { pair_mac(g[2 * i], g[2 * i + 1], xd[i], ag); pair_mac(u[2 * i], u[2 * i + 1], xd[i], au); }
#pragma unroll
        for (int k = 0; k < 8; ++k) { v[k] = ag[k]; v[k + 8] = au[k]; } }
      float A[8], Bq[4]; f32x2 Cq;
#pragma unroll
      for (int k = 0; k < 8; ++k) A[k] = swap32_add(v[k], v[k + 8]);
#pragma unroll
      for (int k = 0; k < 4; ++k) Bq[k] = swap16_add(A[k], A[k + 4]);
      Cq[0] = tstep<XOR8, 8>(Bq[0], Bq[2], lane); Cq[1] = tstep<XOR8, 8>(Bq[1], Bq[3], lane);
      *(f32x2*)(lds + L_F1 + hw * 128 + half * 64 + 8 * ch + 2 * ((lane >> 3) & 1) + 4 * ((lane >> 4) & 1)) = Cq;
      lds_arrive(cnt + 1);
      lds_wait(cnt + 1, 4u, 0);
      const int col = 16 * hw + (lane & 15);
      float gs = 0.f, us = 0.f;
#pragma unroll
      for (int i = 0; i < 4; ++i) { gs += lds[L_F1 + i * 128 + col]; us += lds[L_F1 + i * 128 + 64 + col]; }
      const float hv = gs / (1.f + __expf(-gs)) * us;
      const unsigned hp = cvtpk(hv, dpp<XOR1>(hv));
      float fa[8];
#pragma unroll
      for (int k = 0; k < 8; ++k) fa[k] = 0.f;
#pragma unroll
      for (int i = 0; i < 8; ++i) pair_mac(dn[2 * i], dn[2 * i + 1], __builtin_amdgcn_readlane(hp, 2 * i), fa);
      if (lane < 56) { float* fp = lds + L_FP + hw * 448 + lane * 8; *(f32x4*)fp = f32x4{fa[0], fa[1], fa[2], fa[3]}; *(f32x4*)(fp + 4) = f32x4{fa[4], fa[5], fa[6], fa[7]}; }
    } else {
      if (lane < 56) { float* fp = lds + L_FP + hw * 448 + lane * 8; *(f32x4*)fp = f32x4{0.f, 0.f, 0.f, 0.f}; *(f32x4*)(fp + 4) = f32x4{0.f, 0.f, 0.f, 0.f}; }
    }
    STAMP(2);
#pragma unroll
    for (int i = 0; i < 32; ++i) { wo[i] = LDW(rwo, lane56, (32 * w + i) * (DM * 2)); TRK(); }
    SB();
#pragma unroll
    for (int i = 0; i < 16; ++i) pair_perm(wo[2 * i], wo[2 * i + 1]);
    if (hw == 0) {
      float gp = 0.f;
#pragma unroll
      for (int i = 0; i < 7; ++i) gp += __uint_as_float((unsigned)p.x[b * DM + lane + 64 * i] << 16) * __uint_as_float((unsigned)p.gw[lane + 64 * i] << 16);
      gp = wave_sum(gp);
      const float gate = 1.f / (1.f + __expf(-gp));
      const u64 want = epoch + 1;
#pragma unroll 1
      for (unsigned spins = 0; spins < SPIN_CAP; ++spins) {
        const u64 f = __hip_atomic_load(wsq + (WS_FLAG / 8) + b * 32 + (lane & 31), RLX_AGENT);
        if (__all(f == want)) break;
        __builtin_amdgcn_s_sleep(8);
      }
      if (j == 0 && lane == 0) __hip_atomic_store(wsq + (WS_EPOCH / 8) + b * 32, want, RLX_AGENT);
      lds_wait(cnt + 0, 4u, 1);
      const float mw = lds[L_ML + 2 * (lane & 3)], lw = lds[L_ML + 2 * (lane & 3) + 1];
      float Ms = fmaxf(mw, dpp<XOR1>(mw)); Ms = fmaxf(Ms, dpp<XOR2>(Ms));
      float Ls = lw * __builtin_amdgcn_exp2f((mw - Ms) * C2); Ls += dpp<XOR1>(Ls); Ls += dpp<XOR2>(Ls);
      if (lane < 2) __hip_atomic_store(wsq + (WS_ML / 8) + (b * 32 + j) * 16 + lane, granule(lane ? Ls : Ms), RLX_AGENT);
      u64 gq = 0;
#pragma unroll 1
      for (unsigned spins = 0; spins < SPIN_CAP; ++spins) {
        gq = __hip_atomic_load(wsq + (WS_ML / 8) + (b * 32 + (j ^ 16)) * 16 + (lane & 1), RLX_AGENT);
        if (__all((gq >> 32) == 1ull)) break;
        __builtin_amdgcn_s_sleep(8);
      }
      const float gv = __uint_as_float((unsigned)gq), go = dpp<XOR1>(gv);
      const float Mp = (lane & 1) ? go : gv, Lp = (lane & 1) ? gv : go;
      const float M = fmaxf(Ms, Mp);
      const float L = Ls * __builtin_amdgcn_exp2f((Ms - M) * C2) + Lp * __builtin_amdgcn_exp2f((Mp - M) * C2);
      if (lane < 4) lds[L_FAC + lane] = gate * __builtin_amdgcn_exp2f((mw - M) * C2) / L;
      if (lane == 0) lds[L_FAC + 4] = 1.f - gate;
    }
    STAMP(3);
  }
  wg_barrier();
  STAMP(4);
  {
    const int wp = (lane >> 3) & 3;
    f32x4 o4 = *(const f32x4*)(lds + L_OP + wp * 256 + 32 * w + 4 * (lane & 7));
    const float fc = lds[L_FAC + wp];
#pragma unroll
    for (int k = 0; k < 4; ++k) { float t = o4[k] * fc; t += dpp<XOR8>(t); o4[k] = swap16_add(t, t); }
    const unsigned d0 = cvtpk(o4[0], o4[1]), d1 = cvtpk(o4[2], o4[3]);
    float y[8];
#pragma unroll
    for (int k = 0; k < 8; ++k) y[k] = 0.f;
#pragma unroll
    for (int i = 0; i < 16; ++i) {
      const unsigned op = __builtin_amdgcn_readlane((i & 1) ? d1 : d0, i >> 1);
#pragma unroll
      for (int k = 0; k < 4; ++k) { y[k] = dot2(wo[2 * i][k], op, y[k]); y[4 + k] = dot2(wo[2 * i + 1][k], op, y[4 + k]); }
    }
    if (lane < 56) { float* yp = lds + L_YP + w * 448 + lane * 8; *(f32x4*)yp = f32x4{y[0], y[1], y[2], y[3]}; *(f32x4*)(yp + 4) = f32x4{y[4], y[5], y[6], y[7]}; }
  }
  wg_barrier();
  GAS u64* zq = (GAS u64*)(p.ws + WS_Z);
  if (tid < DM) {
    float z = 0.f, f = 0.f;
#pragma unroll
    for (int i = 0; i < 8; ++i) z += lds[L_YP + i * 448 + tid];
#pragma unroll
    for (int i = 0; i < 4; ++i) f += lds[L_FP + i * 448 + tid];
    z = fmaf(lds[L_FAC + 4], f, z);
    const int c = tid / 14, e = tid - 14 * c;
    __hip_atomic_store(zq + ((size_t)(b * 32 + c) * 32 + j) * 16 + e, granule(z), RLX_AGENT);
  }
  STAMP(5);
  {
    float zv = 0.f;
    const bool act = (tid & 15) < 14;
    GAS u64* src = zq + (size_t)(b * 32 + j) * 512 + tid;
    bool done = !act;
#pragma unroll 1
    for (unsigned spins = 0; spins < SPIN_CAP; ++spins) {
      if (!done) { const u64 g = __hip_atomic_load(src, RLX_AGENT); if ((g >> 32) == 1ull) { zv = __uint_as_float((unsigned)g); done = true; } }
      if (__all(done)) break;
      __builtin_amdgcn_s_sleep(2);
    }
    zv = swap16_add(zv, zv); zv = swap32_add(zv, zv);
    if (lane < 16) lds[L_FIN + w * 16 + lane] = zv;
  }
  STAMP(6);
  wg_barrier();
  if (tid < 14) {
    float o = 0.f;
#pragma unroll
    for (int i = 0; i < 8; ++i) o += lds[L_FIN + i * 16 + tid];
    p.out[b * DM + 14 * j + tid] = (u16)(cvtpk(o, 0.f) & 0xffffu);
  }
  STAMP(7);
  STAMP_FLUSH;
}

extern "C" void kernel_launch(void* const* d_in, const int* in_sizes, int n_in, void* d_out, int out_size, void* d_ws, size_t ws_size, hipStream_t stream) {
  Params p;
  p.x = (const u16*)d_in[0]; p.kc = (const u16*)d_in[1]; p.vc = (const u16*)d_in[2]; p.wq = (const u16*)d_in[3]; p.wo = (const u16*)d_in[4];
  p.wg = (const u16*)d_in[6]; p.wu = (const u16*)d_in[7]; p.wd = (const u16*)d_in[8]; p.gw = (const u16*)d_in[9];
  p.out = (u16*)d_out; p.ws = (unsigned char*)d_ws;
  fused_kernel<<<256, 512, 0, stream>>>(p);
}
```

```cpp
#include <hip/hip_runtime.h>
#include <hip/hip_bf16.h>
#include <stdint.h>
#include <stdio.h>
#include <stdlib.h>
#include <vector>
#include <algorithm>
#define STAMPS
#define NSTAMP 11

typedef unsigned u32x4 __attribute__((ext_vector_type(4)));
typedef unsigned u32x2 __attribute__((ext_vector_type(2)));
typedef float f32x4 __attribute__((ext_vector_type(4)));
typedef float f32x2 __attribute__((ext_vector_type(2)));
typedef short s16x4 __attribute__((ext_vector_type(4)));
typedef short s16x8 __attribute__((ext_vector_type(8)));
typedef __bf16 bf16x2 __attribute__((ext_vector_type(2)));
typedef __bf16 bf16x8 __attribute__((ext_vector_type(8)));
typedef unsigned short u16;
typedef unsigned long long u64;
#define GAS __attribute__((address_space(1)))
#define LAS __attribute__((address_space(3)))
#define RLX_AGENT __ATOMIC_RELAXED, __HIP_MEMORY_SCOPE_AGENT
#define RLX_WG __ATOMIC_RELAXED, __HIP_MEMORY_SCOPE_WORKGROUP

constexpr int NB = 8, NH = 16, SK = 4096, DH = 256, DM = 448, FF = 1792;
#define KF 5
#define QDELAY 10
#define KP 18
#define VX 2
#define HSLEEP 14
#define HSLEEP2 20
constexpr int F = KF, P = KP, R = 32;
constexpr int LS = 8192;
constexpr float C2 = 0.0625f * 1.4426950408889634f;
constexpr unsigned SPIN_CAP = 1u << 18;
constexpr int OOB = 0x7ff00000;

constexpr int WS_EPOCH = 0;
constexpr int WS_Q = 4096;
constexpr int WS_ML = WS_Q + 16 * 8 * 256 * 8;
constexpr int WS_Z = WS_ML + 256 * 128;
constexpr int WS_END = WS_Z + 8 * 32 * 32 * 256;
constexpr size_t WS_STAMP = 4u << 20;
#define STAMP(k) do { ts_[k] = __builtin_amdgcn_s_memrealtime(); } while (0)
#define STAMP_DECL u64 ts_[NSTAMP] = {}
#define STAMP_FLUSH do { if ((threadIdx.x & 63) == 0) { _Pragma("unroll") for (int k_ = 0; k_ < NSTAMP; ++k_) ((u64*)(p.ws + WS_STAMP))[(blockIdx.x * 8 + (threadIdx.x >> 6)) * 16 + k_] = ts_[k_]; } } while (0)

constexpr int L_WS = 0;
constexpr int L_YP = 0;
constexpr int L_P = L_WS + 4 * 32 * 224;
constexpr int L_OP = L_P + 2048;
constexpr int L_FP = L_OP + 1024;
constexpr int L_QS = L_P;
constexpr int L_F1 = L_FP + 4 * 448;
constexpr int L_QR = L_F1 + 512;
constexpr int L_Q = L_QR + 512;
constexpr int L_FIN = L_Q + 128;
constexpr int L_ML = L_FIN + 128;
constexpr int L_FAC = L_ML + 8;
constexpr int L_CNT = L_FAC + 8;
constexpr int L_END = L_CNT + 8;
static_assert(L_QS + 4096 <= L_F1, "wq tiles must fit below L_F1");

struct Params {
  const u16 *x, *kc, *vc, *wq, *wo, *wg, *wu, *wd, *gw;
  u16* out;
  unsigned char* ws;
};

__device__ __forceinline__ float bflo(unsigned v) { return __uint_as_float(v << 16); }
__device__ __forceinline__ float bfhi(unsigned v) { return __uint_as_float(v & 0xffff0000u); }
__device__ __forceinline__ unsigned cvtpk(float lo, float hi) {
  unsigned r; asm("v_cvt_pk_bf16_f32 %0, %1, %2" : "=v"(r) : "v"(lo), "v"(hi)); return r;
}
__device__ __forceinline__ float dot2(unsigned k, unsigned q, float acc) {
  return __builtin_amdgcn_fdot2_f32_bf16(__builtin_bit_cast(bf16x2, k), __builtin_bit_cast(bf16x2, q), acc, false);
}
template <int CTRL> __device__ __forceinline__ float dpp(float x) {
  return __builtin_bit_cast(float, __builtin_amdgcn_mov_dpp(__builtin_bit_cast(int, x), CTRL, 0xf, 0xf, true));
}
constexpr int XOR1 = 0xB1, XOR2 = 0x4E, XOR7 = 0x141, XOR8 = 0x128;
__device__ __forceinline__ float swap16_add(float a, float b) {
  auto s = __builtin_amdgcn_permlane16_swap(__float_as_uint(a), __float_as_uint(b), false, false);
  return __uint_as_float(s[0]) + __uint_as_float(s[1]);
}
__device__ __forceinline__ float swap32_add(float a, float b) {
  auto s = __builtin_amdgcn_permlane32_swap(__float_as_uint(a), __float_as_uint(b), false, false);
  return __uint_as_float(s[0]) + __uint_as_float(s[1]);
}
template <int CTRL, int BIT> __device__ __forceinline__ float tstep(float lo, float hi, int lane) {
  const float t1 = lo + dpp<CTRL>(lo), t2 = hi + dpp<CTRL>(hi);
  return (lane & BIT) ? t2 : t1;
}
__device__ __forceinline__ float wave_max(float x) {
  x = fmaxf(x, dpp<XOR1>(x)); x = fmaxf(x, dpp<XOR2>(x)); x = fmaxf(x, dpp<XOR7>(x)); x = fmaxf(x, dpp<XOR8>(x));
  auto s = __builtin_amdgcn_permlane16_swap(__float_as_uint(x), __float_as_uint(x), false, false);
  x = fmaxf(__uint_as_float(s[0]), __uint_as_float(s[1]));
  auto t = __builtin_amdgcn_permlane32_swap(__float_as_uint(x), __float_as_uint(x), false, false);
  return fmaxf(__uint_as_float(t[0]), __uint_as_float(t[1]));
}
__device__ __forceinline__ float wave_sum(float x) {
  x = x + dpp<XOR1>(x); x = x + dpp<XOR2>(x); x = x + dpp<XOR7>(x); x = x + dpp<XOR8>(x);
  auto s = __builtin_amdgcn_permlane16_swap(__float_as_uint(x), __float_as_uint(x), false, false);
  x = __uint_as_float(s[0]) + __uint_as_float(s[1]);
  auto t = __builtin_amdgcn_permlane32_swap(__float_as_uint(x), __float_as_uint(x), false, false);
  return __uint_as_float(t[0]) + __uint_as_float(t[1]);
}
__device__ __forceinline__ void pair_mac(const u32x4& A, const u32x4& B, unsigned xp, float (&acc)[8]) {
#pragma unroll
  for (int k = 0; k < 4; ++k) {
    const unsigned lo = __builtin_amdgcn_perm(B[k], A[k], 0x05040100u), hi = __builtin_amdgcn_perm(B[k], A[k], 0x07060302u);
    acc[2 * k] = dot2(lo, xp, acc[2 * k]); acc[2 * k + 1] = dot2(hi, xp, acc[2 * k + 1]);
  }
}
__device__ __forceinline__ void pair_perm(u32x4& A, u32x4& B) {
  u32x4 a, b;
  a[0] = __builtin_amdgcn_perm(B[0], A[0], 0x05040100u); a[1] = __builtin_amdgcn_perm(B[0], A[0], 0x07060302u);
  a[2] = __builtin_amdgcn_perm(B[1], A[1], 0x05040100u); a[3] = __builtin_amdgcn_perm(B[1], A[1], 0x07060302u);
  b[0] = __builtin_amdgcn_perm(B[2], A[2], 0x05040100u); b[1] = __builtin_amdgcn_perm(B[2], A[2], 0x07060302u);
  b[2] = __builtin_amdgcn_perm(B[3], A[3], 0x05040100u); b[3] = __builtin_amdgcn_perm(B[3], A[3], 0x07060302u);
  A = a; B = b;
}
#define MKRSRC(ptr, bytes) __builtin_amdgcn_make_buffer_rsrc((void*)(ptr), 0, (int)(bytes), 0x00020000)
#define LDNT(rs, voff, soff) __builtin_amdgcn_raw_buffer_load_b128(rs, voff, soff, 2)
#define LDSC(rs, voff, soff) __builtin_amdgcn_raw_buffer_load_b128(rs, voff, soff, 16)
#define STSC(v, rs, voff) __builtin_amdgcn_raw_buffer_store_b128(v, rs, voff, 0, 16)
#define SB() __builtin_amdgcn_sched_barrier(0)
#define PIN(v) asm volatile("" : "+v"(v))
#define PIN8(a) asm volatile("" : "+v"(a[0]), "+v"(a[1]), "+v"(a[2]), "+v"(a[3]), "+v"(a[4]), "+v"(a[5]), "+v"(a[6]), "+v"(a[7]))
#define TRK(n) do { SB(); __builtin_amdgcn_s_sleep(n); SB(); } while (0)
__device__ __forceinline__ void wg_barrier() {
  asm volatile("s_waitcnt lgkmcnt(0)" ::: "memory");
  __builtin_amdgcn_s_barrier();
  asm volatile("" ::: "memory");
}
__device__ __forceinline__ void lds_arrive(LAS unsigned* c) {
  asm volatile("s_waitcnt lgkmcnt(0)" ::: "memory");
  if ((threadIdx.x & 63) == 0) __hip_atomic_fetch_add(c, 1u, RLX_WG);
}
__device__ __forceinline__ void lds_wait(LAS unsigned* c, unsigned want, int slp) {
#pragma unroll 1
  for (unsigned spins = 0; spins < (1u << 22); ++spins) {
    if (__builtin_amdgcn_readfirstlane(__hip_atomic_load(c, RLX_WG)) >= want) break;
    if (slp) __builtin_amdgcn_s_sleep(16); else __builtin_amdgcn_s_sleep(1);
  }
  asm volatile("" ::: "memory");
}
struct Tag { unsigned lo, hi; };
__device__ __forceinline__ bool gran_ok(unsigned d0, unsigned d1, Tag t) { return (((d0 ^ t.lo) >> 16) | (d1 ^ t.hi)) == 0u; }
__device__ __forceinline__ u32x4 gran_f32(float v, Tag t) { const unsigned u = __float_as_uint(v); return u32x4{t.lo | (u & 0xffffu), t.hi, t.lo | (u >> 16), t.hi}; }
__device__ __forceinline__ bool gran_f32_ok(const u32x4& g, Tag t) { return gran_ok(g[0], g[1], t) && gran_ok(g[2], g[3], t); }
__device__ __forceinline__ float gran_f32_val(const u32x4& g) { return __uint_as_float(__builtin_amdgcn_perm(g[2], g[0], 0x05040100u)); }

__global__ void __launch_bounds__(512, 2) fused_kernel(Params p) {
  __shared__ __attribute__((aligned(16))) float lds[L_END];
  const int tid = threadIdx.x, lane = tid & 63, w = __builtin_amdgcn_readfirstlane(tid >> 6);
  const int j = blockIdx.x & 31, b = blockIdx.x >> 5, h = j & 15, s = j >> 4, half = lane >> 5;
  LAS unsigned* cnt = (LAS unsigned*)(lds + L_CNT);
  STAMP_DECL;
  STAMP(0);
  GAS u64* wsq = (GAS u64*)p.ws;
  const u64 epoch_v = __hip_atomic_load(wsq + WS_EPOCH / 8, RLX_AGENT);
  const __amdgpu_buffer_rsrc_t rws = MKRSRC(p.ws, WS_END);
  u32x4 wo[32];
  const int lane56 = (lane < 56 ? lane : 55) * 16;
  u64 epoch; Tag tag;
#define GET_TAG() { epoch = ((u64)__builtin_amdgcn_readfirstlane((unsigned)(epoch_v >> 32)) << 32) | (unsigned)__builtin_amdgcn_readfirstlane((unsigned)epoch_v); \
    const u64 t48 = ((epoch + 1ull) * 0x9E3779B97F4A7C15ull) >> 16; tag.lo = (unsigned)(t48 << 16); tag.hi = (unsigned)(t48 >> 16); }

  if (w < 4) {
    const size_t base = (size_t)(b * NH + h) * SK * DH;
    const __amdgpu_buffer_rsrc_t rk = MKRSRC(p.kc + base, SK * DH * 2), rv = MKRSRC(p.vc + base, SK * DH * 2);
    const int voff = lane * 16 + w * 2048 + s * 1024;
    u32x4 a[R];
    __builtin_amdgcn_s_barrier();
    SB();
#pragma unroll
    for (int i = 0; i < F; ++i) a[i] = LDNT(rk, voff, i * LS);
    SB();
    __builtin_amdgcn_s_setprio(2);
#pragma unroll
    for (int i = 0; i < P - F; ++i) { PIN(a[i][0]); a[i + F] = LDNT(rk, voff, (i + F) * LS); SB(); }
    STAMP(8);
    GET_TAG();
    lds_wait(cnt + 4, 1u, 0);
    const u32x4 q = *(const u32x4*)(lds + L_Q + (lane & 31) * 4);
    STAMP(1);
    float* pw = lds + L_P + w * 512 + half * 256;
    const int il = ((lane >> 4) & 1) | (((lane >> 3) & 1) << 1) | (((lane >> 2) & 1) << 2) | (((lane >> 1) & 1) << 3) | ((lane & 1) << 4);
    float mx = -INFINITY;
#define KBLOCK(ISSUE, it) { \
      float t1[16], u2[8], u3[4], u4[2], x0 = 0.f; \
      _Pragma("unroll") for (int i = 0; i < 32; ++i) { \
        PIN(a[i][0]); \
        ISSUE; \
        SB(); \
        const u32x4 cur = a[i]; \
        float d = dot2(cur[0], q[0], 0.f); d = dot2(cur[1], q[1], d); d = dot2(cur[2], q[2], d); d = dot2(cur[3], q[3], d); PIN(d); \
        if ((i & 1) == 0) x0 = d; \
        else { t1[i >> 1] = swap16_add(x0, d); \
          if ((i & 3) == 3) { u2[i >> 2] = tstep<XOR8, 8>(t1[(i >> 1) - 1], t1[i >> 1], lane); \
            if ((i & 7) == 7) { u3[i >> 3] = tstep<XOR7, 4>(u2[(i >> 2) - 1], u2[i >> 2], lane); \
              if ((i & 15) == 15) { u4[i >> 4] = tstep<XOR2, 2>(u3[(i >> 3) - 1], u3[i >> 3], lane); \
                if (i == 31) { const float sc = tstep<XOR1, 1>(u4[0], u4[1], lane); mx = fmaxf(mx, sc); pw[(it) * 32 + il] = sc; } } } } } \
        SB(); \
      } }
    KBLOCK(if (i + F >= P) a[(i + F) % R] = LDNT(rk, voff, (i + F) * LS), 0)
#pragma unroll 1
    for (int it = 1; it < 8; ++it) {
      const int so = it * 32 * LS;
      const __amdgpu_buffer_rsrc_t r2 = MKRSRC((it == 7 ? p.vc : p.kc) + base, SK * DH * 2);
      const int so2 = it == 7 ? 0 : so + 32 * LS;
      KBLOCK(if (i + F < 32) a[i + F] = LDNT(rk, voff, so + (i + F) * LS); else a[i + F - 32] = LDNT(r2, voff, so2 + (i + F - 32) * LS), it)
    }
    STAMP(2);
#pragma unroll
    for (int i = F; i < F + VX; ++i) a[i] = LDNT(rv, voff, i * LS);
    SB();
    mx = wave_max(mx);
    float lsum = 0.f;
#pragma unroll
    for (int it = 0; it < 8; ++it) { const float pe = __builtin_amdgcn_exp2f((pw[it * 32 + il] - mx) * C2); lsum += pe; pw[it * 32 + il] = pe; }
    lsum = wave_sum(lsum);
    if (lane == 0) { lds[L_ML + 2 * w] = mx; lds[L_ML + 2 * w + 1] = lsum; }
    lds_arrive(cnt + 0);
    lds_wait(cnt + 2, 4u, 0);
#pragma unroll
    for (int i = 0; i < 32; ++i) wo[i] = *(const u32x4*)((const unsigned char*)(lds + L_WS) + (w * 32 + i) * 896 + lane56);
    float acc[8];
#pragma unroll
    for (int k = 0; k < 8; ++k) acc[k] = 0.f;
    f32x4 pv, pn = *(const f32x4*)pw;
#pragma unroll 1
    for (int it = 0; it < 8; ++it) {
      const int so = it * 32 * LS;
      const int voff2 = it == 7 ? OOB : voff;
#pragma unroll
      for (int i = 0; i < 32; ++i) {
        if ((i & 3) == 0) { pv = pn; pn = *(const f32x4*)(pw + it * 32 + i + 4); }
        PIN(a[i][0]);
        if (i >= VX || it > 0) a[(i + F) % R] = LDNT(rv, i + F < 32 ? voff : voff2, so + (i + F) * LS);
        SB();
        const u32x4 cur = a[i]; const float pe = pv[i & 3];
#pragma unroll
        for (int k = 0; k < 4; ++k) { acc[2 * k] = fmaf(pe, bflo(cur[k]), acc[2 * k]); acc[2 * k + 1] = fmaf(pe, bfhi(cur[k]), acc[2 * k + 1]); }
        PIN8(acc);
        SB();
      }
    }
    STAMP(3);
    f32x4 r;
#pragma unroll
    for (int k = 0; k < 4; ++k) r[k] = swap32_add(acc[k], acc[k + 4]);
    *(f32x4*)(lds + L_OP + w * 256 + (lane & 31) * 8 + half * 4) = r;
  } else {
    const int hw = w - 4;
    if (tid == 256) { cnt[0] = 0u; cnt[1] = 0u; cnt[2] = 0u; cnt[3] = 0u; cnt[4] = 0u; }
    {
      const int c = 2 * b + s;
      const __amdgpu_buffer_rsrc_t rq = MKRSRC(p.wq + h * DH + 16 * c, (size_t)DM * NH * DH * 2 - (h * DH + 16 * c) * 2);
      const __amdgpu_buffer_rsrc_t rx = MKRSRC(p.x, NB * DM * 2);
      const int wv = (lane >> 1) * 8192 + (lane & 1) * 16;
      const int xvo = (lane & 8) ? OOB : (lane & 7) * (DM * 2) + (lane >> 4) * 16;
      u32x4 wt[4], xt[4];
#pragma unroll
      for (int i = 0; i < 4; ++i) {
        const int t = hw + 4 * i;
        wt[i] = LDNT(rq, t < 14 ? wv : OOB, t < 14 ? t * 32 * 8192 : 0);
        xt[i] = __builtin_amdgcn_raw_buffer_load_b128(rx, t < 14 ? xvo : OOB, t < 14 ? t * 64 : 0, 0);
      }
      SB();
      wg_barrier();
      SB();
      float* qs = lds + L_QS + hw * 1024;
#pragma unroll
      for (int i = 0; i < 4; ++i) *(u32x4*)(qs + i * 256 + lane * 4) = wt[i];
      asm volatile("s_waitcnt lgkmcnt(0)" ::: "memory");
      f32x4 d = {0.f, 0.f, 0.f, 0.f};
      const int tro = (8 * (lane >> 4) + ((lane & 15) >> 2)) * 32 + (lane & 3) * 8;
#pragma unroll
      for (int i = 0; i < 4; ++i) {
        LAS unsigned char* tb = (LAS unsigned char*)(qs + i * 256) + tro;
        const s16x4 r0 = __builtin_amdgcn_ds_read_tr16_b64_v4i16((LAS s16x4*)tb);
        const s16x4 r1 = __builtin_amdgcn_ds_read_tr16_b64_v4i16((LAS s16x4*)(tb + 128));
        const s16x8 bb = __builtin_shufflevector(r0, r1, 0, 1, 2, 3, 4, 5, 6, 7);
        d = __builtin_amdgcn_mfma_f32_16x16x32_bf16(__builtin_bit_cast(bf16x8, xt[i]), __builtin_bit_cast(bf16x8, bb), d, 0, 0, 0);
      }
      if (lane < 32) *(f32x4*)(lds + L_QR + hw * 128 + lane * 4) = d;
      lds_arrive(cnt + 3);
      GET_TAG();
      STAMP(8);
      if (hw < 2) {
        lds_wait(cnt + 3, 4u, 0);
        const int t = tid - 256, bb = t >> 4, n = t & 15;
        const float* qr = lds + L_QR + ((bb >> 2) * 16 + n) * 4 + (bb & 3);
        const float qv = (qr[0] + qr[128]) + (qr[256] + qr[384]);
        const u64 g = ((u64)tag.hi << 32) | (tag.lo | (cvtpk(qv, 0.f) & 0xffffu));
        __hip_atomic_store(wsq + WS_Q / 8 + (h * 8 + bb) * 256 + 16 * c + n, g, RLX_AGENT);
      }
      if (hw == 0) {
        __builtin_amdgcn_s_sleep(QDELAY);
        const int qo = WS_Q + ((h * 8 + b) * 256 + 4 * lane) * 8;
        u32x4 g0, g1;
#pragma unroll 1
        for (unsigned spins = 0; spins < SPIN_CAP; ++spins) {
          asm volatile("" ::: "memory");
          g0 = LDSC(rws, qo, 0); g1 = LDSC(rws, qo, 16);
          const bool ok = gran_ok(g0[0], g0[1], tag) && gran_ok(g0[2], g0[3], tag) && gran_ok(g1[0], g1[1], tag) && gran_ok(g1[2], g1[3], tag);
          if (__all(ok)) break;
          ts_[10] += 1;
        }
        *(u32x2*)(lds + L_Q + lane * 2) = u32x2{__builtin_amdgcn_perm(g0[2], g0[0], 0x05040100u), __builtin_amdgcn_perm(g1[2], g1[0], 0x05040100u)};
        lds_arrive(cnt + 4);
      }
    }
    STAMP(1);
    if (j < 28) {
      const int ch = lane & 7, rg = lane >> 3;
      const __amdgpu_buffer_rsrc_t rg_ = MKRSRC(p.wg + 64 * j, (size_t)DM * FF * 2 - 128 * j), ru_ = MKRSRC(p.wu + 64 * j, (size_t)DM * FF * 2 - 128 * j);
      const int voff = (112 * hw + 2 * rg) * (FF * 2) + ch * 16;
      u32x4 g[14], u[14]; unsigned xd[7];
      const unsigned* xp = (const unsigned*)(p.x + b * DM) + 56 * hw + rg;
#pragma unroll
      for (int i = 0; i < 7; ++i) xd[i] = xp[8 * i];
#pragma unroll
      for (int i = 0; i < 7; ++i) {
        g[2 * i] = LDNT(rg_, voff, (16 * i) * (FF * 2)); TRK(HSLEEP); g[2 * i + 1] = LDNT(rg_, voff, (16 * i + 1) * (FF * 2)); TRK(HSLEEP);
        u[2 * i] = LDNT(ru_, voff, (16 * i) * (FF * 2)); TRK(HSLEEP); u[2 * i + 1] = LDNT(ru_, voff, (16 * i + 1) * (FF * 2)); TRK(HSLEEP); }
      const __amdgpu_buffer_rsrc_t rd = MKRSRC(p.wd + (size_t)64 * j * DM, (size_t)(FF - 64 * j) * DM * 2);
      u32x4 dn[16];
#pragma unroll
      for (int i = 0; i < 16; ++i) { dn[i] = LDNT(rd, lane56, (16 * hw + i) * (DM * 2)); TRK(HSLEEP); }
      SB();
      float v[16];
      { float ag[8], au[8];
#pragma unroll
        for (int k = 0; k < 8; ++k) { ag[k] = 0.f; au[k] = 0.f; }
#pragma unroll
        for (int i = 0; i < 7; ++i) { pair_mac(g[2 * i], g[2 * i + 1], xd[i], ag); pair_mac(u[2 * i], u[2 * i + 1], xd[i], au); }
#pragma unroll
        for (int k = 0; k < 8; ++k) { v[k] = ag[k]; v[k + 8] = au[k]; } }
      float A[8], Bq[4]; f32x2 Cq;
#pragma unroll
      for (int k = 0; k < 8; ++k) A[k] = swap32_add(v[k], v[k + 8]);
#pragma unroll
      for (int k = 0; k < 4; ++k) Bq[k] = swap16_add(A[k], A[k + 4]);
      Cq[0] = tstep<XOR8, 8>(Bq[0], Bq[2], lane); Cq[1] = tstep<XOR8, 8>(Bq[1], Bq[3], lane);
      *(f32x2*)(lds + L_F1 + hw * 128 + half * 64 + 8 * ch + 2 * ((lane >> 3) & 1) + 4 * ((lane >> 4) & 1)) = Cq;
      lds_arrive(cnt + 1);
      lds_wait(cnt + 1, 4u, 0);
      const int col = 16 * hw + (lane & 15);
      float gs = 0.f, us = 0.f;
#pragma unroll
      for (int i = 0; i < 4; ++i) { gs += lds[L_F1 + i * 128 + col]; us += lds[L_F1 + i * 128 + 64 + col]; }
      const float hv = gs / (1.f + __expf(-gs)) * us;
      const unsigned hp = cvtpk(hv, dpp<XOR1>(hv));
      float fa[8];
#pragma unroll
      for (int k = 0; k < 8; ++k) fa[k] = 0.f;
#pragma unroll
      for (int i = 0; i < 8; ++i) pair_mac(dn[2 * i], dn[2 * i + 1], __builtin_amdgcn_readlane(hp, 2 * i), fa);
      if (lane < 56) { float* fp = lds + L_FP + hw * 448 + lane * 8; *(f32x4*)fp = f32x4{fa[0], fa[1], fa[2], fa[3]}; *(f32x4*)(fp + 4) = f32x4{fa[4], fa[5], fa[6], fa[7]}; }
    } else {
      if (lane < 56) { float* fp = lds + L_FP + hw * 448 + lane * 8; *(f32x4*)fp = f32x4{0.f, 0.f, 0.f, 0.f}; *(f32x4*)(fp + 4) = f32x4{0.f, 0.f, 0.f, 0.f}; }
    }
    STAMP(2);
    const __amdgpu_buffer_rsrc_t rwo = MKRSRC(p.wo + (size_t)h * DH * DM, (size_t)(NH - h) * DH * DM * 2);
#pragma unroll
    for (int i = 0; i < 32; ++i) { wo[i] = LDNT(rwo, lane56, (32 * hw + i) * (DM * 2)); TRK(HSLEEP); }
    SB();
#pragma unroll
    for (int i = 0; i < 16; ++i) {
      pair_perm(wo[2 * i], wo[2 * i + 1]);
      if (lane < 56) { unsigned char* dst = (unsigned char*)(lds + L_WS) + (hw * 32 + 2 * i) * 896 + lane * 16; *(u32x4*)dst = wo[2 * i]; *(u32x4*)(dst + 896) = wo[2 * i + 1]; }
    }
    lds_arrive(cnt + 2);
    STAMP(9);
    if (hw == 0) {
      float gp = 0.f;
#pragma unroll
      for (int i = 0; i < 7; ++i) gp += __uint_as_float((unsigned)p.x[b * DM + lane + 64 * i] << 16) * __uint_as_float((unsigned)p.gw[lane + 64 * i] << 16);
      gp = wave_sum(gp);
      const float gate = 1.f / (1.f + __expf(-gp));
      lds_wait(cnt + 0, 4u, 1);
      const float mw = lds[L_ML + 2 * (lane & 3)], lw = lds[L_ML + 2 * (lane & 3) + 1];
      float Ms = fmaxf(mw, dpp<XOR1>(mw)); Ms = fmaxf(Ms, dpp<XOR2>(Ms));
      float Ls = lw * __builtin_amdgcn_exp2f((mw - Ms) * C2); Ls += dpp<XOR1>(Ls); Ls += dpp<XOR2>(Ls);
      if (lane < 2) STSC(gran_f32(lane ? Ls : Ms, tag), rws, WS_ML + blockIdx.x * 128 + lane * 16);
      u32x4 gq;
#pragma unroll 1
      for (unsigned spins = 0; spins < SPIN_CAP; ++spins) {
        asm volatile("" ::: "memory");
        gq = LDSC(rws, WS_ML + (blockIdx.x ^ 16) * 128 + (lane & 1) * 16, 0);
        if (__all(gran_f32_ok(gq, tag))) break;
        __builtin_amdgcn_s_sleep(8);
      }
      const float gv = gran_f32_val(gq), go = dpp<XOR1>(gv);
      const float Mp = (lane & 1) ? go : gv, Lp = (lane & 1) ? gv : go;
      const float M = fmaxf(Ms, Mp);
      const float L = Ls * __builtin_amdgcn_exp2f((Ms - M) * C2) + Lp * __builtin_amdgcn_exp2f((Mp - M) * C2);
      if (lane < 4) lds[L_FAC + lane] = gate * __builtin_amdgcn_exp2f((mw - M) * C2) / L;
      if (lane == 0) lds[L_FAC + 4] = 1.f - gate;
    } else {
      lds_wait(cnt + 0, 4u, 1);
    }
#pragma unroll
    for (int i = 0; i < 32; ++i) { wo[i] = LDNT(rwo, lane56, (32 * w + i) * (DM * 2)); TRK(HSLEEP2); }
    SB();
#pragma unroll
    for (int i = 0; i < 16; ++i) pair_perm(wo[2 * i], wo[2 * i + 1]);
    STAMP(3);
  }
  const int zst = WS_Z + (((b * 32 + tid / 14) * 32 + j) * 16 + tid % 14) * 16;
  const int zld = WS_Z + (b * 32 + j) * 8192 + tid * 16;
  wg_barrier();
  STAMP(4);
  {
    const int wp = (lane >> 3) & 3;
    f32x4 o4 = *(const f32x4*)(lds + L_OP + wp * 256 + 32 * w + 4 * (lane & 7));
    const float fc = lds[L_FAC + wp];
#pragma unroll
    for (int k = 0; k < 4; ++k) { float t = o4[k] * fc; t += dpp<XOR8>(t); o4[k] = swap16_add(t, t); }
    const unsigned d0 = cvtpk(o4[0], o4[1]), d1 = cvtpk(o4[2], o4[3]);
    float y[8];
#pragma unroll
    for (int k = 0; k < 8; ++k) y[k] = 0.f;
#pragma unroll
    for (int i = 0; i < 16; ++i) {
      const unsigned op = __builtin_amdgcn_readlane((i & 1) ? d1 : d0, i >> 1);
#pragma unroll
      for (int k = 0; k < 4; ++k) { y[k] = dot2(wo[2 * i][k], op, y[k]); y[4 + k] = dot2(wo[2 * i + 1][k], op, y[4 + k]); }
    }
    if (lane < 56) { float* yp = lds + L_YP + w * 448 + lane * 8; *(f32x4*)yp = f32x4{y[0], y[1], y[2], y[3]}; *(f32x4*)(yp + 4) = f32x4{y[4], y[5], y[6], y[7]}; }
  }
  wg_barrier();
  if (tid < DM) {
    float z = 0.f, f = 0.f;
#pragma unroll
    for (int i = 0; i < 8; ++i) z += lds[L_YP + i * 448 + tid];
#pragma unroll
    for (int i = 0; i < 4; ++i) f += lds[L_FP + i * 448 + tid];
    z = fmaf(lds[L_FAC + 4], f, z);
    STSC(gran_f32(z, tag), rws, zst);
  }
  STAMP(5);
  {
    float zv = 0.f;
    bool done = (tid & 15) >= 14;
#pragma unroll 1
    for (unsigned spins = 0; spins < SPIN_CAP; ++spins) {
      asm volatile("" ::: "memory");
      if (!done) { const u32x4 g = LDSC(rws, zld, 0); if (gran_f32_ok(g, tag)) { zv = gran_f32_val(g); done = true; } }
      if (__all(done)) break;
      __builtin_amdgcn_s_sleep(2);
    }
    zv = swap16_add(zv, zv); zv = swap32_add(zv, zv);
    if (lane < 16) lds[L_FIN + w * 16 + lane] = zv;
  }
  STAMP(6);
  wg_barrier();
  if (tid < 14) {
    float o = 0.f;
#pragma unroll
    for (int i = 0; i < 8; ++i) o += lds[L_FIN + i * 16 + tid];
    p.out[b * DM + 14 * j + tid] = (u16)(cvtpk(o, 0.f) & 0xffffu);
  }
  if (blockIdx.x == 0 && tid == 64) __hip_atomic_store(wsq + WS_EPOCH / 8, epoch + 1ull, RLX_AGENT);
  STAMP(7);
  STAMP_FLUSH;
}

static void* g_ws = nullptr;
static void dump_stamps() {
  if (!g_ws) return;
  std::vector<u64> hs(256 * 8 * 16);
  hipError_t e = hipMemcpy(hs.data(), (char*)g_ws + WS_STAMP, hs.size() * 8, hipMemcpyDeviceToHost);
  fprintf(stderr, "DUMP: memcpy %d\n", (int)e);
  if (e != hipSuccess) return;
  u64 t0 = ~0ull; for (int i = 0; i < 2048; ++i) t0 = std::min(t0, hs[i * 16]);
  for (int k = 0; k < 10; ++k) {
    double ss = 0, sh = 0; float smin = 1e9, smax = -1e9, hmin = 1e9, hmax = -1e9;
    for (int i = 0; i < 2048; ++i) { float t = hs[i * 16 + k] ? (float)(long long)(hs[i * 16 + k] - t0) * 0.01f : 0.f; if ((i & 7) < 4) { ss += t; smin = std::min(smin, t); smax = std::max(smax, t); } else { sh += t; hmin = std::min(hmin, t); hmax = std::max(hmax, t); } }
    fprintf(stderr, "DUMP stamp %d: S min %.2f avg %.2f max %.2f | H min %.2f avg %.2f max %.2f\n", k, smin, ss / 1024, smax, hmin, sh / 1024, hmax);
  }
}
__attribute__((destructor)) static void on_unload() { dump_stamps(); g_ws = nullptr; }
extern "C" void kernel_launch(void* const* d_in, const int* in_sizes, int n_in, void* d_out, int out_size, void* d_ws, size_t ws_size, hipStream_t stream) {
  Params p;
  p.x = (const u16*)d_in[0]; p.kc = (const u16*)d_in[1]; p.vc = (const u16*)d_in[2]; p.wq = (const u16*)d_in[3]; p.wo = (const u16*)d_in[4];
  p.wg = (const u16*)d_in[6]; p.wu = (const u16*)d_in[7]; p.wd = (const u16*)d_in[8]; p.gw = (const u16*)d_in[9];
  p.out = (u16*)d_out; p.ws = (unsigned char*)d_ws;
  fused_kernel<<<256, 512, 0, stream>>>(p);
  g_ws = d_ws;
  static bool reg = false; if (!reg) { reg = true; atexit(dump_stamps); }
}
```

```cpp
#include <hip/hip_runtime.h>
#include <hip/hip_bf16.h>
#include <stdint.h>

typedef unsigned u32x4 __attribute__((ext_vector_type(4)));
typedef unsigned u32x2 __attribute__((ext_vector_type(2)));
typedef float f32x4 __attribute__((ext_vector_type(4)));
typedef float f32x2 __attribute__((ext_vector_type(2)));
typedef short s16x4 __attribute__((ext_vector_type(4)));
typedef short s16x8 __attribute__((ext_vector_type(8)));
typedef __bf16 bf16x2 __attribute__((ext_vector_type(2)));
typedef __bf16 bf16x8 __attribute__((ext_vector_type(8)));
typedef unsigned short u16;
typedef unsigned long long u64;
#define GAS __attribute__((address_space(1)))
#define LAS __attribute__((address_space(3)))
#define RLX_AGENT __ATOMIC_RELAXED, __HIP_MEMORY_SCOPE_AGENT
#define RLX_WG __ATOMIC_RELAXED, __HIP_MEMORY_SCOPE_WORKGROUP

constexpr int NB = 8, NH = 16, SK = 4096, DH = 256, DM = 448, FF = 1792;
#define TAU0 142.5f
#define TOFF 650
#define TADD 20
#define THI 20
#define BIAS 0
#define VX 2
#define QS0 4
#define QS1 5
#define QDELAY 10
#define HSLEEP 20
constexpr int F = 6, P = 32, R = 32;
constexpr unsigned TAU8 = (unsigned)(TAU0 * 12.8f + 0.5f);
constexpr int LS = 8192;
constexpr float C2 = 0.0625f * 1.4426950408889634f;
constexpr unsigned SPIN_CAP = 1u << 18;
constexpr int OOB = 0x7ff00000;

constexpr int NSLOT = 8;
constexpr int WS_EPOCH = 0;
constexpr int WS_Q = 4096;
constexpr int WS_ML = WS_Q + 16 * 8 * 256 * 8;
constexpr int WS_Z = WS_ML + 256 * 128;
constexpr int WS_PROG = WS_Z + 8 * 32 * 32 * 256;
constexpr int WS_END = WS_PROG + 8 * NSLOT * 128;
#define STAMP(k) do {} while (0)
#define STAMP_DECL do {} while (0)
#define DBGF() do {} while (0)
#define STAMP_FLUSH do {} while (0)

constexpr int L_WS = 0;
constexpr int L_YP = 0;
constexpr int L_P = L_WS + 4 * 32 * 224;
constexpr int L_OP = L_P + 2048;
constexpr int L_QS = L_P;
constexpr int L_F1 = L_P + 4096;
constexpr int L_QR = L_F1 + 512;
constexpr int L_Q = L_QR + 512;
constexpr int L_FIN = L_Q + 128;
constexpr int L_ML = L_FIN + 128;
constexpr int L_FAC = L_ML + 8;
constexpr int L_CNT = L_FAC + 8;
constexpr int L_END = L_CNT + 12;

struct Params {
  const u16 *x, *kc, *vc, *wq, *wo, *wg, *wu, *wd, *gw;
  u16* out;
  unsigned char* ws;
};
#define T_TAU8 TAU8
#define T_TOFF TOFF
#define T_THI THI
#define T_TADD TADD
#define T_BIAS ((unsigned)BIAS)

__device__ __forceinline__ float bflo(unsigned v) { return __uint_as_float(v << 16); }
__device__ __forceinline__ float bfhi(unsigned v) { return __uint_as_float(v & 0xffff0000u); }
__device__ __forceinline__ unsigned cvtpk(float lo, float hi) {
  unsigned r; asm("v_cvt_pk_bf16_f32 %0, %1, %2" : "=v"(r) : "v"(lo), "v"(hi)); return r;
}
__device__ __forceinline__ float dot2(unsigned k, unsigned q, float acc) {
  return __builtin_amdgcn_fdot2_f32_bf16(__builtin_bit_cast(bf16x2, k), __builtin_bit_cast(bf16x2, q), acc, false);
}
template <int CTRL> __device__ __forceinline__ float dpp(float x) {
  return __builtin_bit_cast(float, __builtin_amdgcn_mov_dpp(__builtin_bit_cast(int, x), CTRL, 0xf, 0xf, true));
}
constexpr int XOR1 = 0xB1, XOR2 = 0x4E, XOR7 = 0x141, XOR8 = 0x128;
__device__ __forceinline__ float swap16_add(float a, float b) {
  auto s = __builtin_amdgcn_permlane16_swap(__float_as_uint(a), __float_as_uint(b), false, false);
  return __uint_as_float(s[0]) + __uint_as_float(s[1]);
}
__device__ __forceinline__ float swap32_add(float a, float b) {
  auto s = __builtin_amdgcn_permlane32_swap(__float_as_uint(a), __float_as_uint(b), false, false);
  return __uint_as_float(s[0]) + __uint_as_float(s[1]);
}
template <int CTRL, int BIT> __device__ __forceinline__ float tstep(float lo, float hi, int lane) {
  const float t1 = lo + dpp<CTRL>(lo), t2 = hi + dpp<CTRL>(hi);
  return (lane & BIT) ? t2 : t1;
}
__device__ __forceinline__ float wave_max(float x) {
  x = fmaxf(x, dpp<XOR1>(x)); x = fmaxf(x, dpp<XOR2>(x)); x = fmaxf(x, dpp<XOR7>(x)); x = fmaxf(x, dpp<XOR8>(x));
  auto s = __builtin_amdgcn_permlane16_swap(__float_as_uint(x), __float_as_uint(x), false, false);
  x = fmaxf(__uint_as_float(s[0]), __uint_as_float(s[1]));
  auto t = __builtin_amdgcn_permlane32_swap(__float_as_uint(x), __float_as_uint(x), false, false);
  return fmaxf(__uint_as_float(t[0]), __uint_as_float(t[1]));
}
__device__ __forceinline__ float wave_sum(float x) {
  x = x + dpp<XOR1>(x); x = x + dpp<XOR2>(x); x = x + dpp<XOR7>(x); x = x + dpp<XOR8>(x);
  auto s = __builtin_amdgcn_permlane16_swap(__float_as_uint(x), __float_as_uint(x), false, false);
  x = __uint_as_float(s[0]) + __uint_as_float(s[1]);
  auto t = __builtin_amdgcn_permlane32_swap(__float_as_uint(x), __float_as_uint(x), false, false);
  return __uint_as_float(t[0]) + __uint_as_float(t[1]);
}
__device__ __forceinline__ void pair_mac(const u32x4& A, const u32x4& B, unsigned xp, float (&acc)[8]) {
#pragma unroll
  for (int k = 0; k < 4; ++k) {
    const unsigned lo = __builtin_amdgcn_perm(B[k], A[k], 0x05040100u), hi = __builtin_amdgcn_perm(B[k], A[k], 0x07060302u);
    acc[2 * k] = dot2(lo, xp, acc[2 * k]); acc[2 * k + 1] = dot2(hi, xp, acc[2 * k + 1]);
  }
}
__device__ __forceinline__ void pair_perm(u32x4& A, u32x4& B) {
  u32x4 a, b;
  a[0] = __builtin_amdgcn_perm(B[0], A[0], 0x05040100u); a[1] = __builtin_amdgcn_perm(B[0], A[0], 0x07060302u);
  a[2] = __builtin_amdgcn_perm(B[1], A[1], 0x05040100u); a[3] = __builtin_amdgcn_perm(B[1], A[1], 0x07060302u);
  b[0] = __builtin_amdgcn_perm(B[2], A[2], 0x05040100u); b[1] = __builtin_amdgcn_perm(B[2], A[2], 0x07060302u);
  b[2] = __builtin_amdgcn_perm(B[3], A[3], 0x05040100u); b[3] = __builtin_amdgcn_perm(B[3], A[3], 0x07060302u);
  A = a; B = b;
}
#define MKRSRC(ptr, bytes) __builtin_amdgcn_make_buffer_rsrc((void*)(ptr), 0, (int)(bytes), 0x00020000)
#define LDNT(rs, voff, soff) __builtin_amdgcn_raw_buffer_load_b128(rs, voff, soff, 2)
#define LDSC(rs, voff, soff) __builtin_amdgcn_raw_buffer_load_b128(rs, voff, soff, 16)
#define STSC(v, rs, voff) __builtin_amdgcn_raw_buffer_store_b128(v, rs, voff, 0, 16)
#define SB() __builtin_amdgcn_sched_barrier(0)
#define PIN(v) asm volatile("" : "+v"(v))
#define PIN8(a) asm volatile("" : "+v"(a[0]), "+v"(a[1]), "+v"(a[2]), "+v"(a[3]), "+v"(a[4]), "+v"(a[5]), "+v"(a[6]), "+v"(a[7]))
#define NAP(n) do { SB(); __builtin_amdgcn_s_sleep(n); SB(); } while (0)
#define UNI(x) __builtin_amdgcn_readfirstlane(x)
__device__ __forceinline__ void wg_barrier() {
  asm volatile("s_waitcnt lgkmcnt(0)" ::: "memory");
  __builtin_amdgcn_s_barrier();
  asm volatile("" ::: "memory");
}
__device__ __forceinline__ unsigned lds_get(LAS unsigned* c) { return UNI(__hip_atomic_load(c, RLX_WG)); }
__device__ __forceinline__ void lds_arrive(LAS unsigned* c) {
  asm volatile("s_waitcnt lgkmcnt(0)" ::: "memory");
  if ((threadIdx.x & 63) == 0) __hip_atomic_fetch_add(c, 1u, RLX_WG);
}
__device__ __forceinline__ void lds_wait(LAS unsigned* c, unsigned want, int slp) {
#pragma unroll 1
  for (unsigned spins = 0; spins < (1u << 22); ++spins) {
    if (lds_get(c) >= want) break;
    if (slp) __builtin_amdgcn_s_sleep(16); else __builtin_amdgcn_s_sleep(1);
  }
  asm volatile("" ::: "memory");
}
struct Tag { unsigned lo, hi; };
__device__ __forceinline__ bool gran_ok(unsigned d0, unsigned d1, Tag t) { return (((d0 ^ t.lo) >> 16) | (d1 ^ t.hi)) == 0u; }
__device__ __forceinline__ u64 gran16(unsigned v, Tag t) { return ((u64)t.hi << 32) | (t.lo | (v & 0xffffu)); }
__device__ __forceinline__ u32x4 gran_f32(float v, Tag t) { const unsigned u = __float_as_uint(v); return u32x4{t.lo | (u & 0xffffu), t.hi, t.lo | (u >> 16), t.hi}; }
__device__ __forceinline__ bool gran_f32_ok(const u32x4& g, Tag t) { return gran_ok(g[0], g[1], t) && gran_ok(g[2], g[3], t); }
__device__ __forceinline__ float gran_f32_val(const u32x4& g) { return __uint_as_float(__builtin_amdgcn_perm(g[2], g[0], 0x05040100u)); }
constexpr bool siteP(int n) { return n >= 8 && (n - 8) % 16 == 0 && (n - 8) / 16 < NSLOT - 1; }
constexpr bool siteC(int n) { return n >= 12 && (n - 12) % 16 == 0 && (n - 12) / 16 < NSLOT - 1; }
constexpr bool siteSkip(int n) { return siteC(n - 1) || siteC(n - 2) || siteC(n - 3); }

__global__ void __launch_bounds__(512, 2) fused_kernel(Params p) {
  __shared__ __attribute__((aligned(16))) float lds[L_END];
  const int tid = threadIdx.x, lane = tid & 63, w = UNI(tid >> 6);
  const int j = blockIdx.x & 31, b = blockIdx.x >> 5, h = j & 15, s = j >> 4, half = lane >> 5;
  LAS unsigned* cnt = (LAS unsigned*)(lds + L_CNT);
  STAMP_DECL;
  STAMP(0);
  if (tid < 8) cnt[tid] = 0u;
  wg_barrier();
  GAS u64* wsq = (GAS u64*)p.ws;
  const __amdgpu_buffer_rsrc_t rws = MKRSRC(p.ws, WS_END);
  u32x4 wo[32];
  float y[8];
  const int lane56 = (lane < 56 ? lane : 55) * 16;
  Tag tag;

  if (w < 4) {
    const size_t base = (size_t)(b * NH + h) * SK * DH;
    const __amdgpu_buffer_rsrc_t rk = MKRSRC(p.kc + base, SK * DH * 2), rv = MKRSRC(p.vc + base, SK * DH * 2);
    const int voff = lane * 16 + w * 2048 + s * 1024;
    u32x4 a[R];
#pragma unroll
    for (int i = 0; i < F - 1; ++i) a[i] = LDNT(rk, voff, i * LS);
    SB();
    __builtin_amdgcn_s_setprio(2);
    unsigned ref = (unsigned)__builtin_amdgcn_s_memrealtime() + T_TOFF, nom = 0u;
    int f = 5;
#define ADAPT(FIRST) do { unsigned r_ = 0u; if (FIRST) r_ = __hip_atomic_load(cnt + 6, RLX_WG); \
      const unsigned ph_ = (unsigned)__builtin_amdgcn_s_memrealtime() - (nom >> 4); nom += T_TAU8; \
      if (FIRST) { r_ = UNI(r_); if (r_ >> 31) ref = r_; if (w == 0 && lane == 0) __hip_atomic_store(cnt + 5, (ph_ & 0xffffu) | 0x80000000u, RLX_WG); } \
      const int lag = (int)(short)(ph_ - ref); f = lag > T_THI ? 6 : 5; DBGF(); } while (0)
#define LIMIT() asm volatile("s_cmp_eq_u32 %0, 6\n\ts_cbranch_scc1 1f\n\ts_waitcnt vmcnt(4)\n1:" :: "s"(f) : "scc")
    PIN(a[0][0]); a[F - 1] = LDNT(rk, voff, (F - 1) * LS); SB();
#pragma unroll
    for (int i = 0; i < P - F; ++i) { PIN(a[i][0]); LIMIT(); a[i + F] = LDNT(rk, voff, (i + F) * LS); SB(); }
    STAMP(8);
    lds_wait(cnt + 4, 1u, 0);
    const u32x4 q = *(const u32x4*)(lds + L_Q + (lane & 31) * 4);
    STAMP(1);
    float* pw = lds + L_P + w * 512 + half * 256;
    const int il = ((lane >> 4) & 1) | (((lane >> 3) & 1) << 1) | (((lane >> 2) & 1) << 2) | (((lane >> 1) & 1) << 3) | ((lane & 1) << 4);
    float mx = -INFINITY;
#define KBLOCK(ISSUE, LATE, it) { \
      float t1[16], u2[8], u3[4], u4[2], x0 = 0.f; \
      _Pragma("unroll") for (int i = 0; i < 32; ++i) { \
        ISSUE; \
        SB(); \
        const u32x4 cur = a[i]; \
        float d = dot2(cur[0], q[0], 0.f); d = dot2(cur[1], q[1], d); d = dot2(cur[2], q[2], d); d = dot2(cur[3], q[3], d); PIN(d); \
        if ((i & 1) == 0) x0 = d; \
        else { t1[i >> 1] = swap16_add(x0, d); \
          if ((i & 3) == 3) { u2[i >> 2] = tstep<XOR8, 8>(t1[(i >> 1) - 1], t1[i >> 1], lane); \
            if ((i & 7) == 7) { u3[i >> 3] = tstep<XOR7, 4>(u2[(i >> 2) - 1], u2[i >> 2], lane); \
              if ((i & 15) == 15) { u4[i >> 4] = tstep<XOR2, 2>(u3[(i >> 3) - 1], u3[i >> 3], lane); \
                if (i == 31) { const float sc = tstep<XOR1, 1>(u4[0], u4[1], lane); mx = fmaxf(mx, sc); pw[(it) * 32 + il] = sc; } } } } } \
        SB(); \
        LATE; \
      } }
    KBLOCK(PIN(a[i][0]), if (i % 5 == 0 && i / 5 < F) { LIMIT(); a[i / 5] = LDNT(rk, voff, (32 + i / 5) * LS); SB(); }, 0)
#pragma unroll 1
    for (int it = 1; it < 7; ++it) {
      const int so = it * 32 * LS;
      KBLOCK(if ((i & 7) == 0) ADAPT(i == 0); PIN(a[i][0]); LIMIT(); a[(i + F) % R] = LDNT(rk, voff, so + (i + F) * LS), , it)
    }
    lds_wait(cnt + 2, 4u, 0);
    KBLOCK(if ((i & 7) == 0) ADAPT(i == 0); PIN(a[i][0]); LIMIT();
           if (i + F < 32) a[i + F] = LDNT(rk, voff, (7 * 32 + i + F) * LS); else a[i + F - 32] = LDNT(rv, voff, (i + F - 32) * LS),
           wo[i] = *(const u32x4*)((const unsigned char*)(lds + L_WS) + (w * 32 + i) * 896 + lane56); PIN(wo[i][0]), 7)
    STAMP(2);
    nom += (unsigned)T_TADD << 4;
#pragma unroll
    for (int i = F; i < F + VX; ++i) a[i] = LDNT(rv, voff, i * LS);
    SB();
    mx = wave_max(mx);
    float lsum = 0.f;
#pragma unroll
    for (int it = 0; it < 8; ++it) { const float pe = __builtin_amdgcn_exp2f((pw[it * 32 + il] - mx) * C2); lsum += pe; pw[it * 32 + il] = pe; }
    lsum = wave_sum(lsum);
    if (lane == 0) { lds[L_ML + 2 * w] = mx; lds[L_ML + 2 * w + 1] = lsum; }
    lds_arrive(cnt + 0);
    tag.lo = lds_get(cnt + 7); tag.hi = lds_get(cnt + 8);
    float acc[8];
#pragma unroll
    for (int k = 0; k < 8; ++k) acc[k] = 0.f;
    f32x4 pv, pn = *(const f32x4*)pw;
#pragma unroll 1
    for (int it = 0; it < 8; ++it) {
      const int so = it * 32 * LS;
      const int voff2 = it == 7 ? OOB : voff;
#pragma unroll
      for (int i = 0; i < 32; ++i) {
        if ((i & 3) == 0) { pv = pn; pn = *(const f32x4*)(pw + it * 32 + i + 4); }
        if ((i & 7) == 0) ADAPT(i == 0);
        PIN(a[i][0]);
        if (i >= VX || it > 0) { LIMIT(); a[(i + F) % R] = LDNT(rv, i + F < 32 ? voff : voff2, so + (i + F) * LS); }
        SB();
        const u32x4 cur = a[i]; const float pe = pv[i & 3];
#pragma unroll
        for (int k = 0; k < 4; ++k) { acc[2 * k] = fmaf(pe, bflo(cur[k]), acc[2 * k]); acc[2 * k + 1] = fmaf(pe, bfhi(cur[k]), acc[2 * k + 1]); }
        PIN8(acc);
        SB();
      }
    }
    STAMP(3);
    f32x4 r;
#pragma unroll
    for (int k = 0; k < 4; ++k) r[k] = swap32_add(acc[k], acc[k + 4]);
    *(f32x4*)(lds + L_OP + w * 256 + (lane & 31) * 8 + half * 4) = r;
#pragma unroll
    for (int k = 0; k < 8; ++k) y[k] = 0.f;
  } else {
    const int hw = w - 4;
    const u64 epoch_v = __hip_atomic_load(wsq + WS_EPOCH / 8, RLX_AGENT);
    u64 epoch;
    {
      const int c = 2 * b + s;
      const __amdgpu_buffer_rsrc_t rq = MKRSRC(p.wq + h * DH + 16 * c, (size_t)DM * NH * DH * 2 - (h * DH + 16 * c) * 2);
      const __amdgpu_buffer_rsrc_t rx = MKRSRC(p.x, NB * DM * 2);
      const int wv = (lane >> 1) * 8192 + (lane & 1) * 16;
      const int xvo = (lane & 8) ? OOB : (lane & 7) * (DM * 2) + (lane >> 4) * 16;
      u32x4 wt[4], xt[4];
      NAP(QS0);
#pragma unroll
      for (int i = 0; i < 4; ++i) {
        const int t = hw + 4 * i;
        xt[i] = __builtin_amdgcn_raw_buffer_load_b128(rx, t < 14 ? xvo : OOB, t < 14 ? t * 64 : 0, 0);
        wt[i] = LDNT(rq, t < 14 ? wv : OOB, t < 14 ? t * 32 * 8192 : 0);
        if (i < 3) NAP(QS1);
      }
      SB();
      epoch = ((u64)UNI((unsigned)(epoch_v >> 32)) << 32) | (unsigned)UNI((unsigned)epoch_v);
      { const u64 t48 = ((epoch + 1ull) * 0x9E3779B97F4A7C15ull) >> 16; tag.lo = (unsigned)(t48 << 16); tag.hi = (unsigned)(t48 >> 16); }
      if (tid == 256) { cnt[7] = tag.lo; cnt[8] = tag.hi; }
      float* qs = lds + L_QS + hw * 1024;
#pragma unroll
      for (int i = 0; i < 4; ++i) *(u32x4*)(qs + i * 256 + lane * 4) = wt[i];
      asm volatile("s_waitcnt lgkmcnt(0)" ::: "memory");
      f32x4 d = {0.f, 0.f, 0.f, 0.f};
      const int tro = (8 * (lane >> 4) + ((lane & 15) >> 2)) * 32 + (lane & 3) * 8;
#pragma unroll
      for (int i = 0; i < 4; ++i) {
        LAS unsigned char* tb = (LAS unsigned char*)(qs + i * 256) + tro;
        const s16x4 r0 = __builtin_amdgcn_ds_read_tr16_b64_v4i16((LAS s16x4*)tb);
        const s16x4 r1 = __builtin_amdgcn_ds_read_tr16_b64_v4i16((LAS s16x4*)(tb + 128));
        const s16x8 bb = __builtin_shufflevector(r0, r1, 0, 1, 2, 3, 4, 5, 6, 7);
        d = __builtin_amdgcn_mfma_f32_16x16x32_bf16(__builtin_bit_cast(bf16x8, xt[i]), __builtin_bit_cast(bf16x8, bb), d, 0, 0, 0);
      }
      if (lane < 32) *(f32x4*)(lds + L_QR + hw * 128 + lane * 4) = d;
      lds_arrive(cnt + 3);
      STAMP(8);
      if (hw < 2) {
        lds_wait(cnt + 3, 4u, 0);
        const int t = tid - 256, bb = t >> 4, n = t & 15;
        const float* qr = lds + L_QR + ((bb >> 2) * 16 + n) * 4 + (bb & 3);
        const float qv = (qr[0] + qr[128]) + (qr[256] + qr[384]);
        __hip_atomic_store(wsq + WS_Q / 8 + (h * 8 + bb) * 256 + 16 * c + n, gran16(cvtpk(qv, 0.f), tag), RLX_AGENT);
      }
      if (hw == 0) {
        __builtin_amdgcn_s_sleep(QDELAY);
        const int qo = WS_Q + ((h * 8 + b) * 256 + 4 * lane) * 8;
        u32x4 g0, g1;
#pragma unroll 1
        for (unsigned spins = 0; spins < SPIN_CAP; ++spins) {
          asm volatile("" ::: "memory");
          g0 = LDSC(rws, qo, 0); g1 = LDSC(rws, qo, 16);
          const bool ok = gran_ok(g0[0], g0[1], tag) && gran_ok(g0[2], g0[3], tag) && gran_ok(g1[0], g1[1], tag) && gran_ok(g1[2], g1[3], tag);
          if (__all(ok)) break;
        }
        *(u32x2*)(lds + L_Q + lane * 2) = u32x2{__builtin_amdgcn_perm(g0[2], g0[0], 0x05040100u), __builtin_amdgcn_perm(g1[2], g1[0], 0x05040100u)};
        lds_arrive(cnt + 4);
      }
    }
    STAMP(1);
    typedef const __attribute__((address_space(4))) u64* cptr_t;
#define SITE(n) do { if (siteP(n) && hw == 3 && blockIdx.x < 8) { const unsigned ph_ = lds_get(cnt + 5); \
        if ((ph_ >> 31) && lane == 0) __hip_atomic_store(wsq + WS_PROG / 8 + (blockIdx.x * NSLOT + ((n) - 8) / 16) * 16, gran16(ph_, tag), RLX_AGENT); } \
      if (siteC(n) && hw == 3) { \
        cptr_t pp_ = (cptr_t)(p.ws + WS_PROG + (((n) - 12) / 16) * 128); asm volatile("" : "+s"(pp_)); \
        int sum_ = 0, n_ = 0; unsigned base_ = 0; \
        _Pragma("unroll") for (int k_ = 0; k_ < 8; ++k_) { const u64 e_ = pp_[k_ * (NSLOT * 16)]; \
          const bool ok_ = gran_ok((unsigned)e_, (unsigned)(e_ >> 32), tag); \
          if (ok_ && n_ == 0) base_ = (unsigned)e_; \
          sum_ += ok_ ? (int)(short)((unsigned)e_ - base_) : 0; n_ += ok_ ? 1 : 0; } \
        if (n_ >= 6 && lane == 0) __hip_atomic_store(cnt + 6, ((base_ + (unsigned)(int)((float)sum_ / (float)n_) - T_BIAS) & 0xffffu) | 0x80000000u, RLX_WG); } } while (0)
#define TRK(n) do { SB(); if (!(siteSkip(n) && hw == 3)) __builtin_amdgcn_s_sleep(HSLEEP); SB(); SITE(n); } while (0)
    const __amdgpu_buffer_rsrc_t rwo = MKRSRC(p.wo + (size_t)h * DH * DM, (size_t)(NH - h) * DH * DM * 2);
#pragma unroll
    for (int i = 0; i < 32; ++i) { wo[i] = LDNT(rwo, lane56, (32 * hw + i) * (DM * 2)); TRK(i); }
    SB();
#pragma unroll
    for (int i = 0; i < 16; ++i) {
      pair_perm(wo[2 * i], wo[2 * i + 1]);
      if (lane < 56) { unsigned char* dst = (unsigned char*)(lds + L_WS) + (hw * 32 + 2 * i) * 896 + lane * 16; *(u32x4*)dst = wo[2 * i]; *(u32x4*)(dst + 896) = wo[2 * i + 1]; }
    }
    lds_arrive(cnt + 2);
#pragma unroll
    for (int k = 0; k < 8; ++k) y[k] = 0.f;
    if (j < 28) {
      const int ch = lane & 7, rg = lane >> 3;
      const __amdgpu_buffer_rsrc_t rg_ = MKRSRC(p.wg + 64 * j, (size_t)DM * FF * 2 - 128 * j), ru_ = MKRSRC(p.wu + 64 * j, (size_t)DM * FF * 2 - 128 * j);
      const int voff = (112 * hw + 2 * rg) * (FF * 2) + ch * 16;
      u32x4 g[14], u[14]; unsigned xd[7];
      const unsigned* xp = (const unsigned*)(p.x + b * DM) + 56 * hw + rg;
#pragma unroll
      for (int i = 0; i < 7; ++i) xd[i] = xp[8 * i];
#pragma unroll
      for (int i = 0; i < 7; ++i) {
        g[2 * i] = LDNT(rg_, voff, (16 * i) * (FF * 2)); TRK(32 + 4 * i); g[2 * i + 1] = LDNT(rg_, voff, (16 * i + 1) * (FF * 2)); TRK(33 + 4 * i);
        u[2 * i] = LDNT(ru_, voff, (16 * i) * (FF * 2)); TRK(34 + 4 * i); u[2 * i + 1] = LDNT(ru_, voff, (16 * i + 1) * (FF * 2)); TRK(35 + 4 * i); }
      const __amdgpu_buffer_rsrc_t rd = MKRSRC(p.wd + (size_t)64 * j * DM, (size_t)(FF - 64 * j) * DM * 2);
      u32x4 dn[16];
#pragma unroll
      for (int i = 0; i < 16; ++i) { dn[i] = LDNT(rd, lane56, (16 * hw + i) * (DM * 2)); TRK(60 + i); }
      SB();
      float v[16];
      { float ag[8], au[8];
#pragma unroll
        for (int k = 0; k < 8; ++k) { ag[k] = 0.f; au[k] = 0.f; }
#pragma unroll
        for (int i = 0; i < 7; ++i) { pair_mac(g[2 * i], g[2 * i + 1], xd[i], ag); pair_mac(u[2 * i], u[2 * i + 1], xd[i], au); }
#pragma unroll
        for (int k = 0; k < 8; ++k) { v[k] = ag[k]; v[k + 8] = au[k]; } }
      float A[8], Bq[4]; f32x2 Cq;
#pragma unroll
      for (int k = 0; k < 8; ++k) A[k] = swap32_add(v[k], v[k + 8]);
#pragma unroll
      for (int k = 0; k < 4; ++k) Bq[k] = swap16_add(A[k], A[k + 4]);
      Cq[0] = tstep<XOR8, 8>(Bq[0], Bq[2], lane); Cq[1] = tstep<XOR8, 8>(Bq[1], Bq[3], lane);
      *(f32x2*)(lds + L_F1 + hw * 128 + half * 64 + 8 * ch + 2 * ((lane >> 3) & 1) + 4 * ((lane >> 4) & 1)) = Cq;
      lds_arrive(cnt + 1);
      lds_wait(cnt + 1, 4u, 0);
      const int col = 16 * hw + (lane & 15);
      float gs = 0.f, us = 0.f;
#pragma unroll
      for (int i = 0; i < 4; ++i) { gs += lds[L_F1 + i * 128 + col]; us += lds[L_F1 + i * 128 + 64 + col]; }
      const float hv = gs / (1.f + __expf(-gs)) * us;
      const unsigned hp = cvtpk(hv, dpp<XOR1>(hv));
#pragma unroll
      for (int i = 0; i < 8; ++i) pair_mac(dn[2 * i], dn[2 * i + 1], __builtin_amdgcn_readlane(hp, 2 * i), y);
    } else {
#pragma unroll
      for (int n = 32; n < 76; ++n) TRK(n);
    }
    STAMP(2);
    {
      float gp = 0.f;
#pragma unroll
      for (int i = 0; i < 7; ++i) gp += __uint_as_float((unsigned)p.x[b * DM + lane + 64 * i] << 16) * __uint_as_float((unsigned)p.gw[lane + 64 * i] << 16);
      gp = wave_sum(gp);
      const float gate = 1.f / (1.f + __expf(-gp));
#pragma unroll
      for (int k = 0; k < 8; ++k) y[k] *= 1.f - gate;
      if (hw == 0) {
        lds_wait(cnt + 0, 4u, 1);
        const float mw = lds[L_ML + 2 * (lane & 3)], lw = lds[L_ML + 2 * (lane & 3) + 1];
        float Ms = fmaxf(mw, dpp<XOR1>(mw)); Ms = fmaxf(Ms, dpp<XOR2>(Ms));
        float Ls = lw * __builtin_amdgcn_exp2f((mw - Ms) * C2); Ls += dpp<XOR1>(Ls); Ls += dpp<XOR2>(Ls);
        if (lane < 2) STSC(gran_f32(lane ? Ls : Ms, tag), rws, WS_ML + blockIdx.x * 128 + lane * 16);
        u32x4 gq;
#pragma unroll 1
        for (unsigned spins = 0; spins < SPIN_CAP; ++spins) {
          asm volatile("" ::: "memory");
          gq = LDSC(rws, WS_ML + (blockIdx.x ^ 16) * 128 + (lane & 1) * 16, 0);
          if (__all(gran_f32_ok(gq, tag))) break;
          __builtin_amdgcn_s_sleep(8);
        }
        const float gv = gran_f32_val(gq), go = dpp<XOR1>(gv);
        const float Mp = (lane & 1) ? go : gv, Lp = (lane & 1) ? gv : go;
        const float M = fmaxf(Ms, Mp);
        const float L = Ls * __builtin_amdgcn_exp2f((Ms - M) * C2) + Lp * __builtin_amdgcn_exp2f((Mp - M) * C2);
        if (lane < 4) lds[L_FAC + lane] = gate * __builtin_amdgcn_exp2f((mw - M) * C2) / L;
      }
    }
#pragma unroll
    for (int i = 0; i < 32; ++i) { wo[i] = LDNT(rwo, lane56, (32 * w + i) * (DM * 2)); TRK(76 + i); }
    SB();
#pragma unroll
    for (int i = 0; i < 16; ++i) pair_perm(wo[2 * i], wo[2 * i + 1]);
    PIN8(y);
    if (blockIdx.x == 0 && tid == 256) lds[L_FAC + 4] = __uint_as_float((unsigned)((epoch + 1ull) >> 32)), lds[L_FAC + 5] = __uint_as_float((unsigned)(epoch + 1ull));
    STAMP(3);
  }
  const int zst = WS_Z + (((b * 32 + tid / 14) * 32 + j) * 16 + tid % 14) * 16;
  const int zld = WS_Z + (b * 32 + j) * 8192 + tid * 16;
  wg_barrier();
  STAMP(4);
  {
    const int wp = (lane >> 3) & 3;
    f32x4 o4 = *(const f32x4*)(lds + L_OP + wp * 256 + 32 * w + 4 * (lane & 7));
    const float fc = lds[L_FAC + wp];
#pragma unroll
    for (int k = 0; k < 4; ++k) { float t = o4[k] * fc; t += dpp<XOR8>(t); o4[k] = swap16_add(t, t); }
    const unsigned d0 = cvtpk(o4[0], o4[1]), d1 = cvtpk(o4[2], o4[3]);
#pragma unroll
    for (int i = 0; i < 16; ++i) {
      const unsigned op = __builtin_amdgcn_readlane((i & 1) ? d1 : d0, i >> 1);
#pragma unroll
      for (int k = 0; k < 4; ++k) { y[k] = dot2(wo[2 * i][k], op, y[k]); y[4 + k] = dot2(wo[2 * i + 1][k], op, y[4 + k]); }
    }
    if (lane < 56) { float* yp = lds + L_YP + w * 448 + lane * 8; *(f32x4*)yp = f32x4{y[0], y[1], y[2], y[3]}; *(f32x4*)(yp + 4) = f32x4{y[4], y[5], y[6], y[7]}; }
  }
  wg_barrier();
  if (tid < DM) {
    float z = 0.f;
#pragma unroll
    for (int i = 0; i < 8; ++i) z += lds[L_YP + i * 448 + tid];
    STSC(gran_f32(z, tag), rws, zst);
  }
  STAMP(5);
  {
    float zv = 0.f;
    bool done = (tid & 15) >= 14;
    u32x4 ga = LDSC(rws, zld, 0), gb;
#pragma unroll 1
    for (unsigned spins = 0; spins < SPIN_CAP; ++spins) {
      __builtin_amdgcn_s_sleep(4);
      asm volatile("" ::: "memory");
      gb = LDSC(rws, zld, 0);
      if (!done && gran_f32_ok(ga, tag)) { zv = gran_f32_val(ga); done = true; }
      if (__all(done)) break;
      __builtin_amdgcn_s_sleep(4);
      asm volatile("" ::: "memory");
      ga = LDSC(rws, zld, 0);
      if (!done && gran_f32_ok(gb, tag)) { zv = gran_f32_val(gb); done = true; }
      if (__all(done)) break;
    }
    zv = swap16_add(zv, zv); zv = swap32_add(zv, zv);
    if (lane < 16) lds[L_FIN + w * 16 + lane] = zv;
  }
  STAMP(6);
  wg_barrier();
  if (tid < 14) {
    float o = 0.f;
#pragma unroll
    for (int i = 0; i < 8; ++i) o += lds[L_FIN + i * 16 + tid];
    p.out[b * DM + 14 * j + tid] = (u16)(cvtpk(o, 0.f) & 0xffffu);
  }
  if (blockIdx.x == 0 && tid == 64) __hip_atomic_store(wsq + WS_EPOCH / 8, ((u64)__float_as_uint(lds[L_FAC + 4]) << 32) | __float_as_uint(lds[L_FAC + 5]), RLX_AGENT);
  STAMP(7);
  STAMP_FLUSH;
}

extern "C" void kernel_launch(void* const* d_in, const int* in_sizes, int n_in, void* d_out, int out_size, void* d_ws, size_t ws_size, hipStream_t stream) {
  Params p;
  p.x = (const u16*)d_in[0]; p.kc = (const u16*)d_in[1]; p.vc = (const u16*)d_in[2]; p.wq = (const u16*)d_in[3]; p.wo = (const u16*)d_in[4];
  p.wg = (const u16*)d_in[6]; p.wu = (const u16*)d_in[7]; p.wd = (const u16*)d_in[8]; p.gw = (const u16*)d_in[9];
  p.out = (u16*)d_out; p.ws = (unsigned char*)d_ws;
  fused_kernel<<<256, 512, 0, stream>>>(p);
}
```

```cpp
#include <hip/hip_runtime.h>
#include <hip/hip_bf16.h>
#include <stdint.h>

typedef unsigned u32x4 __attribute__((ext_vector_type(4)));
typedef unsigned u32x2 __attribute__((ext_vector_type(2)));
typedef float f32x4 __attribute__((ext_vector_type(4)));
typedef float f32x2 __attribute__((ext_vector_type(2)));
typedef short s16x4 __attribute__((ext_vector_type(4)));
typedef short s16x8 __attribute__((ext_vector_type(8)));
typedef __bf16 bf16x2 __attribute__((ext_vector_type(2)));
typedef __bf16 bf16x8 __attribute__((ext_vector_type(8)));
typedef unsigned short u16;
typedef unsigned long long u64;
#define GAS __attribute__((address_space(1)))
#define LAS __attribute__((address_space(3)))
#define RLX_AGENT __ATOMIC_RELAXED, __HIP_MEMORY_SCOPE_AGENT
#define RLX_WG __ATOMIC_RELAXED, __HIP_MEMORY_SCOPE_WORKGROUP

constexpr int NB = 8, NH = 16, SK = 4096, DH = 256, DM = 448, FF = 1792;
constexpr float TAU0 = 142.5f;
constexpr int TOFF = 580;
constexpr int TADD = 20;
constexpr int THI = 20;
constexpr int VX = 2;
constexpr unsigned HT0 = 560;
constexpr unsigned HPER = 56;
constexpr unsigned HLEAD = 90;
constexpr int QS0 = 4, QS1 = 5, QDELAY = 10, PINIT = 20, PGAP = 3;
constexpr int F = 6, P = 32, R = 32;
constexpr unsigned TAU8 = (unsigned)(TAU0 * 12.8f + 0.5f);
constexpr int LS = 8192;
constexpr float C2 = 0.0625f * 1.4426950408889634f;
constexpr unsigned SPIN_CAP = 1u << 18;
constexpr int OOB = 0x7ff00000;

constexpr int NSLOT = 8;
constexpr int WS_EPOCH = 0;
constexpr int WS_Q = 4096;
constexpr int WS_ML = WS_Q + 16 * 8 * 256 * 8;
constexpr int WS_Z = WS_ML + 256 * 128;
constexpr int WS_PROG = WS_Z + 8 * 32 * 32 * 256;
constexpr int WS_END = WS_PROG + 8 * NSLOT * 128;

constexpr int L_WS = 0;
constexpr int L_YP = 0;
constexpr int L_P = L_WS + 4 * 32 * 224;
constexpr int L_OP = L_P + 2048;
constexpr int L_QS = L_P;
constexpr int L_F1 = L_P + 4096;
constexpr int L_QR = L_F1 + 512;
constexpr int L_Q = L_QR + 512;
constexpr int L_FIN = L_Q + 128;
constexpr int L_ML = L_FIN + 128;
constexpr int L_FAC = L_ML + 8;
constexpr int L_CNT = L_FAC + 8;
constexpr int L_END = L_CNT + 12;

struct Params {
  const u16 *x, *kc, *vc, *wq, *wo, *wg, *wu, *wd, *gw;
  u16* out;
  unsigned char* ws;
};

__device__ __forceinline__ float bflo(unsigned v) { return __uint_as_float(v << 16); }
__device__ __forceinline__ float bfhi(unsigned v) { return __uint_as_float(v & 0xffff0000u); }
__device__ __forceinline__ unsigned cvtpk(float lo, float hi) {
  unsigned r; asm("v_cvt_pk_bf16_f32 %0, %1, %2" : "=v"(r) : "v"(lo), "v"(hi)); return r;
}
__device__ __forceinline__ float dot2(unsigned k, unsigned q, float acc) {
  return __builtin_amdgcn_fdot2_f32_bf16(__builtin_bit_cast(bf16x2, k), __builtin_bit_cast(bf16x2, q), acc, false);
}
template <int CTRL> __device__ __forceinline__ float dpp(float x) {
  return __builtin_bit_cast(float, __builtin_amdgcn_mov_dpp(__builtin_bit_cast(int, x), CTRL, 0xf, 0xf, true));
}
constexpr int XOR1 = 0xB1, XOR2 = 0x4E, XOR7 = 0x141, XOR8 = 0x128;
__device__ __forceinline__ float swap16_add(float a, float b) {
  auto s = __builtin_amdgcn_permlane16_swap(__float_as_uint(a), __float_as_uint(b), false, false);
  return __uint_as_float(s[0]) + __uint_as_float(s[1]);
}
__device__ __forceinline__ float swap32_add(float a, float b) {
  auto s = __builtin_amdgcn_permlane32_swap(__float_as_uint(a), __float_as_uint(b), false, false);
  return __uint_as_float(s[0]) + __uint_as_float(s[1]);
}
template <int CTRL, int BIT> __device__ __forceinline__ float tstep(float lo, float hi, int lane) {
  const float t1 = lo + dpp<CTRL>(lo), t2 = hi + dpp<CTRL>(hi);
  return (lane & BIT) ? t2 : t1;
}
__device__ __forceinline__ float wave_max(float x) {
  x = fmaxf(x, dpp<XOR1>(x)); x = fmaxf(x, dpp<XOR2>(x)); x = fmaxf(x, dpp<XOR7>(x)); x = fmaxf(x, dpp<XOR8>(x));
  auto s = __builtin_amdgcn_permlane16_swap(__float_as_uint(x), __float_as_uint(x), false, false);
  x = fmaxf(__uint_as_float(s[0]), __uint_as_float(s[1]));
  auto t = __builtin_amdgcn_permlane32_swap(__float_as_uint(x), __float_as_uint(x), false, false);
  return fmaxf(__uint_as_float(t[0]), __uint_as_float(t[1]));
}
__device__ __forceinline__ float wave_sum(float x) {
  x = x + dpp<XOR1>(x); x = x + dpp<XOR2>(x); x = x + dpp<XOR7>(x); x = x + dpp<XOR8>(x);
  auto s = __builtin_amdgcn_permlane16_swap(__float_as_uint(x), __float_as_uint(x), false, false);
  x = __uint_as_float(s[0]) + __uint_as_float(s[1]);
  auto t = __builtin_amdgcn_permlane32_swap(__float_as_uint(x), __float_as_uint(x), false, false);
  return __uint_as_float(t[0]) + __uint_as_float(t[1]);
}
__device__ __forceinline__ void pair_mac(const u32x4& A, const u32x4& B, unsigned xp, float (&acc)[8]) {
#pragma unroll
  for (int k = 0; k < 4; ++k) {
    const unsigned lo = __builtin_amdgcn_perm(B[k], A[k], 0x05040100u), hi = __builtin_amdgcn_perm(B[k], A[k], 0x07060302u);
    acc[2 * k] = dot2(lo, xp, acc[2 * k]); acc[2 * k + 1] = dot2(hi, xp, acc[2 * k + 1]);
  }
}
__device__ __forceinline__ void pair_perm(u32x4& A, u32x4& B) {
  u32x4 a, b;
  a[0] = __builtin_amdgcn_perm(B[0], A[0], 0x05040100u); a[1] = __builtin_amdgcn_perm(B[0], A[0], 0x07060302u);
  a[2] = __builtin_amdgcn_perm(B[1], A[1], 0x05040100u); a[3] = __builtin_amdgcn_perm(B[1], A[1], 0x07060302u);
  b[0] = __builtin_amdgcn_perm(B[2], A[2], 0x05040100u); b[1] = __builtin_amdgcn_perm(B[2], A[2], 0x07060302u);
  b[2] = __builtin_amdgcn_perm(B[3], A[3], 0x05040100u); b[3] = __builtin_amdgcn_perm(B[3], A[3], 0x07060302u);
  A = a; B = b;
}
#define MKRSRC(ptr, bytes) __builtin_amdgcn_make_buffer_rsrc((void*)(ptr), 0, (int)(bytes), 0x00020000)
#define LDNT(rs, voff, soff) __builtin_amdgcn_raw_buffer_load_b128(rs, voff, soff, 2)
#define LDSC(rs, voff, soff) __builtin_amdgcn_raw_buffer_load_b128(rs, voff, soff, 16)
#define STSC(v, rs, voff) __builtin_amdgcn_raw_buffer_store_b128(v, rs, voff, 0, 16)
#define SB() __builtin_amdgcn_sched_barrier(0)
#define PIN(v) asm volatile("" : "+v"(v))
#define PIN8(a) asm volatile("" : "+v"(a[0]), "+v"(a[1]), "+v"(a[2]), "+v"(a[3]), "+v"(a[4]), "+v"(a[5]), "+v"(a[6]), "+v"(a[7]))
#define NAP(n) do { SB(); __builtin_amdgcn_s_sleep(n); SB(); } while (0)
#define UNI(x) __builtin_amdgcn_readfirstlane(x)
__device__ __forceinline__ void wg_barrier() {
  asm volatile("s_waitcnt lgkmcnt(0)" ::: "memory");
  __builtin_amdgcn_s_barrier();
  asm volatile("" ::: "memory");
}
__device__ __forceinline__ unsigned lds_get(LAS unsigned* c) { return UNI(__hip_atomic_load(c, RLX_WG)); }
__device__ __forceinline__ void lds_arrive(LAS unsigned* c) {
  asm volatile("s_waitcnt lgkmcnt(0)" ::: "memory");
  if ((threadIdx.x & 63) == 0) __hip_atomic_fetch_add(c, 1u, RLX_WG);
}
__device__ __forceinline__ void lds_wait(LAS unsigned* c, unsigned want, int slp) {
#pragma unroll 1
  for (unsigned spins = 0; spins < (1u << 22); ++spins) {
    if (lds_get(c) >= want) break;
    if (slp) __builtin_amdgcn_s_sleep(16); else __builtin_amdgcn_s_sleep(1);
  }
  asm volatile("" ::: "memory");
}
struct Tag { unsigned lo, hi; };
__device__ __forceinline__ bool gran_ok(unsigned d0, unsigned d1, Tag t) { return (((d0 ^ t.lo) >> 16) | (d1 ^ t.hi)) == 0u; }
__device__ __forceinline__ u64 gran16(unsigned v, Tag t) { return ((u64)t.hi << 32) | (t.lo | (v & 0xffffu)); }
__device__ __forceinline__ u32x4 gran_f32(float v, Tag t) { const unsigned u = __float_as_uint(v); return u32x4{t.lo | (u & 0xffffu), t.hi, t.lo | (u >> 16), t.hi}; }
__device__ __forceinline__ bool gran_f32_ok(const u32x4& g, Tag t) { return gran_ok(g[0], g[1], t) && gran_ok(g[2], g[3], t); }
__device__ __forceinline__ float gran_f32_val(const u32x4& g) { return __uint_as_float(__builtin_amdgcn_perm(g[2], g[0], 0x05040100u)); }
constexpr bool siteP(int n) { return n >= 8 && (n - 8) % 16 == 0 && (n - 8) / 16 < NSLOT - 1; }
constexpr bool siteC(int n) { return n >= 12 && (n - 12) % 16 == 0 && (n - 12) / 16 < NSLOT - 1; }

__global__ void __launch_bounds__(512, 2) fused_kernel(Params p) {
  __shared__ __attribute__((aligned(16))) float lds[L_END];
  const int tid = threadIdx.x, lane = tid & 63, w = UNI(tid >> 6);
  const int j = blockIdx.x & 31, b = blockIdx.x >> 5, h = j & 15, s = j >> 4, half = lane >> 5;
  LAS unsigned* cnt = (LAS unsigned*)(lds + L_CNT);
  if (tid < 8) cnt[tid] = 0u;
  GAS u64* wsq = (GAS u64*)p.ws;
  const __amdgpu_buffer_rsrc_t rws = MKRSRC(p.ws, WS_END);
  u32x4 wo[32];
  float y[8];
  const int lane56 = (lane < 56 ? lane : 55) * 16;
  Tag tag;

  if (__builtin_expect(w < 4, 1)) {
    const size_t base = (size_t)(b * NH + h) * SK * DH;
    const __amdgpu_buffer_rsrc_t rk = MKRSRC(p.kc + base, SK * DH * 2), rv = MKRSRC(p.vc + base, SK * DH * 2);
    const int voff = lane * 16 + w * 2048 + s * 1024;
    u32x4 a[R];
#pragma unroll
    for (int i = 0; i < F - 1; ++i) a[i] = LDNT(rk, voff, i * LS);
    SB();
    wg_barrier();
    __builtin_amdgcn_s_setprio(2);
    unsigned ref = (unsigned)__builtin_amdgcn_s_memrealtime() + TOFF - (TAU8 >> 2), nom = 0u;
    int f = 5;
#define ADAPT(FIRST) do { unsigned r_ = 0u; if (FIRST) r_ = __hip_atomic_load(cnt + 6, RLX_WG); \
      const unsigned ph_ = (unsigned)__builtin_amdgcn_s_memrealtime() - (nom >> 4); nom += TAU8; \
      if (FIRST) { r_ = UNI(r_); if (r_ >> 31) ref = r_; if (w == 0 && lane == 0) __hip_atomic_store(cnt + 5, (ph_ & 0xffffu) | 0x80000000u, RLX_WG); } \
      const int lag = (int)(short)(ph_ - ref); f = lag > THI ? 6 : 5; } while (0)
#define LIMIT() asm volatile("s_cmp_eq_u32 %0, 6\n\ts_cbranch_scc1 1f\n\ts_waitcnt vmcnt(4)\n1:" :: "s"(f) : "scc")
    PIN(a[0][0]); a[F - 1] = LDNT(rk, voff, (F - 1) * LS); SB();
#pragma unroll
    for (int i = 0; i < P - F; ++i) { if ((i & 7) == 0) ADAPT(false); PIN(a[i][0]); LIMIT(); a[i + F] = LDNT(rk, voff, (i + F) * LS); SB(); }
    lds_wait(cnt + 4, 1u, 0);
    const u32x4 q = *(const u32x4*)(lds + L_Q + (lane & 31) * 4);
    float* pw = lds + L_P + w * 512 + half * 256;
    const int il = ((lane >> 4) & 1) | (((lane >> 3) & 1) << 1) | (((lane >> 2) & 1) << 2) | (((lane >> 1) & 1) << 3) | ((lane & 1) << 4);
    float mx = -INFINITY;
#define KBLOCK(ISSUE, LATE, it) { \
      float t1[16], u2[8], u3[4], u4[2], x0 = 0.f; \
      _Pragma("unroll") for (int i = 0; i < 32; ++i) { \
        ISSUE; \
        SB(); \
        const u32x4 cur = a[i]; \
        float d = dot2(cur[0], q[0], 0.f); d = dot2(cur[1], q[1], d); d = dot2(cur[2], q[2], d); d = dot2(cur[3], q[3], d); PIN(d); \
        if ((i & 1) == 0) x0 = d; \
        else { t1[i >> 1] = swap16_add(x0, d); \
          if ((i & 3) == 3) { u2[i >> 2] = tstep<XOR8, 8>(t1[(i >> 1) - 1], t1[i >> 1], lane); \
            if ((i & 7) == 7) { u3[i >> 3] = tstep<XOR7, 4>(u2[(i >> 2) - 1], u2[i >> 2], lane); \
              if ((i & 15) == 15) { u4[i >> 4] = tstep<XOR2, 2>(u3[(i >> 3) - 1], u3[i >> 3], lane); \
                if (i == 31) { const float sc = tstep<XOR1, 1>(u4[0], u4[1], lane); mx = fmaxf(mx, sc); pw[(it) * 32 + il] = sc; } } } } } \
        SB(); \
        LATE; \
      } }
    KBLOCK(PIN(a[i][0]), if (i % 6 == 1) { LIMIT(); a[i / 6] = LDNT(rk, voff, (32 + i / 6) * LS); SB(); }, 0)
#pragma unroll 1
    for (int it = 1; it < 7; ++it) {
      const int so = it * 32 * LS;
      KBLOCK(if ((i & 7) == 0) ADAPT(i == 0); PIN(a[i][0]); LIMIT(); a[(i + F) % R] = LDNT(rk, voff, so + (i + F) * LS), , it)
    }
    lds_wait(cnt + 2, 4u, 0);
    KBLOCK(if ((i & 7) == 0) ADAPT(i == 0); PIN(a[i][0]); LIMIT();
           if (i + F < 32) a[i + F] = LDNT(rk, voff, (7 * 32 + i + F) * LS); else a[i + F - 32] = LDNT(rv, voff, (i + F - 32) * LS),
           wo[i] = *(const u32x4*)((const unsigned char*)(lds + L_WS) + (w * 32 + i) * 896 + lane56); PIN(wo[i][0]), 7)
    nom += (unsigned)TADD << 4;
#pragma unroll
    for (int i = F; i < F + VX; ++i) a[i] = LDNT(rv, voff, i * LS);
    SB();
    mx = wave_max(mx);
    float lsum = 0.f;
#pragma unroll
    for (int it = 0; it < 8; ++it) { const float pe = __builtin_amdgcn_exp2f((pw[it * 32 + il] - mx) * C2); lsum += pe; pw[it * 32 + il] = pe; }
    lsum = wave_sum(lsum);
    if (lane == 0) { lds[L_ML + 2 * w] = mx; lds[L_ML + 2 * w + 1] = lsum; }
    lds_arrive(cnt + 0);
    tag.lo = lds_get(cnt + 7); tag.hi = lds_get(cnt + 8);
    float acc[8];
#pragma unroll
    for (int k = 0; k < 8; ++k) acc[k] = 0.f;
    f32x4 pv, pn = *(const f32x4*)pw;
#pragma unroll 1
    for (int it = 0; it < 8; ++it) {
      const int so = it * 32 * LS;
      const int voff2 = it == 7 ? OOB : voff;
#pragma unroll
      for (int i = 0; i < 32; ++i) {
        if ((i & 3) == 0) { pv = pn; pn = *(const f32x4*)(pw + it * 32 + i + 4); }
        if ((i & 7) == 0) ADAPT(i == 0);
        PIN(a[i][0]);
        if (i >= VX || it > 0) { LIMIT(); a[(i + F) % R] = LDNT(rv, i + F < 32 ? voff : voff2, so + (i + F) * LS); }
        SB();
        const u32x4 cur = a[i]; const float pe = pv[i & 3];
#pragma unroll
        for (int k = 0; k < 4; ++k) { acc[2 * k] = fmaf(pe, bflo(cur[k]), acc[2 * k]); acc[2 * k + 1] = fmaf(pe, bfhi(cur[k]), acc[2 * k + 1]); }
        PIN8(acc);
        SB();
      }
    }
    __builtin_amdgcn_s_setprio(0);
    f32x4 r;
#pragma unroll
    for (int k = 0; k < 4; ++k) r[k] = swap32_add(acc[k], acc[k + 4]);
    *(f32x4*)(lds + L_OP + w * 256 + (lane & 31) * 8 + half * 4) = r;
#pragma unroll
    for (int k = 0; k < 8; ++k) y[k] = 0.f;
  } else {
    const int hw = w - 4;
    unsigned htgt = (unsigned)__builtin_amdgcn_s_memrealtime() + HT0;
    wg_barrier();
    const u64 epoch_v = *(const __attribute__((address_space(4))) u64*)(p.ws + WS_EPOCH);
    u64 epoch;
    {
      const int c = 2 * b + s;
      const __amdgpu_buffer_rsrc_t rq = MKRSRC(p.wq + h * DH + 16 * c, (size_t)DM * NH * DH * 2 - (h * DH + 16 * c) * 2);
      const __amdgpu_buffer_rsrc_t rx = MKRSRC(p.x, NB * DM * 2);
      const int wv = (lane >> 1) * 8192 + (lane & 1) * 16;
      const int xvo = (lane & 8) ? OOB : (lane & 7) * (DM * 2) + (lane >> 4) * 16;
      u32x4 wt[4], xt[4];
      NAP(QS0);
#pragma unroll
      for (int i = 0; i < 4; ++i) {
        const int t = hw + 4 * i;
        xt[i] = __builtin_amdgcn_raw_buffer_load_b128(rx, t < 14 ? xvo : OOB, t < 14 ? t * 64 : 0, 0);
        wt[i] = LDNT(rq, t < 14 ? wv : OOB, t < 14 ? t * 32 * 8192 : 0);
        if (i < 3) NAP(QS1);
      }
      SB();
      epoch = epoch_v;
      { const u64 t48 = ((epoch + 1ull) * 0x9E3779B97F4A7C15ull) >> 16; tag.lo = (unsigned)(t48 << 16); tag.hi = (unsigned)(t48 >> 16); }
      if (tid == 256) { cnt[7] = tag.lo; cnt[8] = tag.hi; }
      float* qs = lds + L_QS + hw * 1024;
#pragma unroll
      for (int i = 0; i < 4; ++i) *(u32x4*)(qs + i * 256 + lane * 4) = wt[i];
      asm volatile("s_waitcnt lgkmcnt(0)" ::: "memory");
      f32x4 d = {0.f, 0.f, 0.f, 0.f};
      const int tro = (8 * (lane >> 4) + ((lane & 15) >> 2)) * 32 + (lane & 3) * 8;
#pragma unroll
      for (int i = 0; i < 4; ++i) {
        LAS unsigned char* tb = (LAS unsigned char*)(qs + i * 256) + tro;
        const s16x4 r0 = __builtin_amdgcn_ds_read_tr16_b64_v4i16((LAS s16x4*)tb);
        const s16x4 r1 = __builtin_amdgcn_ds_read_tr16_b64_v4i16((LAS s16x4*)(tb + 128));
        const s16x8 bb = __builtin_shufflevector(r0, r1, 0, 1, 2, 3, 4, 5, 6, 7);
        d = __builtin_amdgcn_mfma_f32_16x16x32_bf16(__builtin_bit_cast(bf16x8, xt[i]), __builtin_bit_cast(bf16x8, bb), d, 0, 0, 0);
      }
      if (lane < 32) *(f32x4*)(lds + L_QR + hw * 128 + lane * 4) = d;
      lds_arrive(cnt + 3);
      if (hw < 2) {
        lds_wait(cnt + 3, 4u, 0);
        const int t = tid - 256, bb = t >> 4, n = t & 15;
        const float* qr = lds + L_QR + ((bb >> 2) * 16 + n) * 4 + (bb & 3);
        const float qv = (qr[0] + qr[128]) + (qr[256] + qr[384]);
        __hip_atomic_store(wsq + WS_Q / 8 + (h * 8 + bb) * 256 + 16 * c + n, gran16(cvtpk(qv, 0.f), tag), RLX_AGENT);
      }
      if (hw == 0) {
        __builtin_amdgcn_s_sleep(QDELAY);
        const int qo = WS_Q + ((h * 8 + b) * 256 + 4 * lane) * 8;
        u32x4 g0, g1;
#pragma unroll 1
        for (unsigned spins = 0; spins < SPIN_CAP; ++spins) {
          asm volatile("" ::: "memory");
          g0 = LDSC(rws, qo, 0); g1 = LDSC(rws, qo, 16);
          const bool ok = gran_ok(g0[0], g0[1], tag) && gran_ok(g0[2], g0[3], tag) && gran_ok(g1[0], g1[1], tag) && gran_ok(g1[2], g1[3], tag);
          if (__all(ok)) break;
        }
        *(u32x2*)(lds + L_Q + lane * 2) = u32x2{__builtin_amdgcn_perm(g0[2], g0[0], 0x05040100u), __builtin_amdgcn_perm(g1[2], g1[0], 0x05040100u)};
        lds_arrive(cnt + 4);
      }
    }
    typedef const __attribute__((address_space(4))) u64* cptr_t;
#define SITE(n) do { if (siteP(n) && hw == 3 && blockIdx.x < 8) { const unsigned ph_ = lds_get(cnt + 5); \
        if ((ph_ >> 31) && lane == 0) __hip_atomic_store(wsq + WS_PROG / 8 + (blockIdx.x * NSLOT + ((n) - 8) / 16) * 16, gran16(ph_, tag), RLX_AGENT); } \
      if (siteC(n) && hw == 3) { \
        cptr_t pp_ = (cptr_t)(p.ws + WS_PROG + (((n) - 12) / 16) * 128); asm volatile("" : "+s"(pp_)); \
        int sum_ = 0, n_ = 0; unsigned base_ = 0; \
        _Pragma("unroll") for (int k_ = 0; k_ < 8; ++k_) { const u64 e_ = pp_[k_ * (NSLOT * 16)]; \
          const bool ok_ = gran_ok((unsigned)e_, (unsigned)(e_ >> 32), tag); \
          if (ok_ && n_ == 0) base_ = (unsigned)e_; \
          sum_ += ok_ ? (int)(short)((unsigned)e_ - base_) : 0; n_ += ok_ ? 1 : 0; } \
        if (n_ >= 6 && lane == 0) __hip_atomic_store(cnt + 6, ((base_ + (unsigned)(int)((float)sum_ / (float)n_)) & 0xffffu) | 0x80000000u, RLX_WG); } } while (0)
#define PRE(n) do { SB(); const unsigned tg_ = htgt + (b != ((n) & 7) ? HLEAD : 0u); htgt += HPER; \
      for (;;) { const int rem_ = (int)(tg_ - (unsigned)__builtin_amdgcn_s_memrealtime()); if (rem_ <= 0) break; if (rem_ > 24) __builtin_amdgcn_s_sleep(8); else __builtin_amdgcn_s_sleep(1); } SB(); } while (0)
#define TRK(n) do { SB(); SITE(n); } while (0)
    const __amdgpu_buffer_rsrc_t rwo = MKRSRC(p.wo + (size_t)h * DH * DM, (size_t)(NH - h) * DH * DM * 2);
#pragma unroll
    for (int i = 0; i < 32; ++i) { PRE(i); wo[i] = LDNT(rwo, lane56, (32 * hw + i) * (DM * 2)); TRK(i); }
    SB();
#pragma unroll
    for (int i = 0; i < 16; ++i) {
      pair_perm(wo[2 * i], wo[2 * i + 1]);
      if (lane < 56) { unsigned char* dst = (unsigned char*)(lds + L_WS) + (hw * 32 + 2 * i) * 896 + lane * 16; *(u32x4*)dst = wo[2 * i]; *(u32x4*)(dst + 896) = wo[2 * i + 1]; }
    }
    lds_arrive(cnt + 2);
#pragma unroll
    for (int k = 0; k < 8; ++k) y[k] = 0.f;
    if (j < 28) {
      const int ch = lane & 7, rg = lane >> 3;
      const __amdgpu_buffer_rsrc_t rg_ = MKRSRC(p.wg + 64 * j, (size_t)DM * FF * 2 - 128 * j), ru_ = MKRSRC(p.wu + 64 * j, (size_t)DM * FF * 2 - 128 * j);
      const int voff = (112 * hw + 2 * rg) * (FF * 2) + ch * 16;
      u32x4 g[14], u[14]; unsigned xd[7];
      const unsigned* xp = (const unsigned*)(p.x + b * DM) + 56 * hw + rg;
#pragma unroll
      for (int i = 0; i < 7; ++i) xd[i] = xp[8 * i];
#pragma unroll
      for (int i = 0; i < 7; ++i) {
        PRE(32 + 4 * i); g[2 * i] = LDNT(rg_, voff, (16 * i) * (FF * 2)); TRK(32 + 4 * i); PRE(33 + 4 * i); g[2 * i + 1] = LDNT(rg_, voff, (16 * i + 1) * (FF * 2)); TRK(33 + 4 * i);
        PRE(34 + 4 * i); u[2 * i] = LDNT(ru_, voff, (16 * i) * (FF * 2)); TRK(34 + 4 * i); PRE(35 + 4 * i); u[2 * i + 1] = LDNT(ru_, voff, (16 * i + 1) * (FF * 2)); TRK(35 + 4 * i); }
      const __amdgpu_buffer_rsrc_t rd = MKRSRC(p.wd + (size_t)64 * j * DM, (size_t)(FF - 64 * j) * DM * 2);
      u32x4 dn[16];
#pragma unroll
      for (int i = 0; i < 16; ++i) { PRE(60 + i); dn[i] = LDNT(rd, lane56, (16 * hw + i) * (DM * 2)); TRK(60 + i); }
      SB();
      float v[16];
      { float ag[8], au[8];
#pragma unroll
        for (int k = 0; k < 8; ++k) { ag[k] = 0.f; au[k] = 0.f; }
#pragma unroll
        for (int i = 0; i < 7; ++i) { pair_mac(g[2 * i], g[2 * i + 1], xd[i], ag); pair_mac(u[2 * i], u[2 * i + 1], xd[i], au); }
#pragma unroll
        for (int k = 0; k < 8; ++k) { v[k] = ag[k]; v[k + 8] = au[k]; } }
      float A[8], Bq[4]; f32x2 Cq;
#pragma unroll
      for (int k = 0; k < 8; ++k) A[k] = swap32_add(v[k], v[k + 8]);
#pragma unroll
      for (int k = 0; k < 4; ++k) Bq[k] = swap16_add(A[k], A[k + 4]);
      Cq[0] = tstep<XOR8, 8>(Bq[0], Bq[2], lane); Cq[1] = tstep<XOR8, 8>(Bq[1], Bq[3], lane);
      *(f32x2*)(lds + L_F1 + hw * 128 + half * 64 + 8 * ch + 2 * ((lane >> 3) & 1) + 4 * ((lane >> 4) & 1)) = Cq;
      lds_arrive(cnt + 1);
      lds_wait(cnt + 1, 4u, 0);
      const int col = 16 * hw + (lane & 15);
      float gs = 0.f, us = 0.f;
#pragma unroll
      for (int i = 0; i < 4; ++i) { gs += lds[L_F1 + i * 128 + col]; us += lds[L_F1 + i * 128 + 64 + col]; }
      const float hv = gs / (1.f + __expf(-gs)) * us;
      const unsigned hp = cvtpk(hv, dpp<XOR1>(hv));
#pragma unroll
      for (int i = 0; i < 8; ++i) pair_mac(dn[2 * i], dn[2 * i + 1], __builtin_amdgcn_readlane(hp, 2 * i), y);
    } else {
#pragma unroll
      for (int n = 32; n < 76; ++n) { PRE(n); TRK(n); }
    }
    {
      float gp = 0.f;
#pragma unroll
      for (int i = 0; i < 7; ++i) gp += __uint_as_float((unsigned)p.x[b * DM + lane + 64 * i] << 16) * __uint_as_float((unsigned)p.gw[lane + 64 * i] << 16);
      gp = wave_sum(gp);
      const float gate = 1.f / (1.f + __expf(-gp));
#pragma unroll
      for (int k = 0; k < 8; ++k) y[k] *= 1.f - gate;
      if (hw == 0) {
        lds_wait(cnt + 0, 4u, 1);
        const float mw = lds[L_ML + 2 * (lane & 3)], lw = lds[L_ML + 2 * (lane & 3) + 1];
        float Ms = fmaxf(mw, dpp<XOR1>(mw)); Ms = fmaxf(Ms, dpp<XOR2>(Ms));
        float Ls = lw * __builtin_amdgcn_exp2f((mw - Ms) * C2); Ls += dpp<XOR1>(Ls); Ls += dpp<XOR2>(Ls);
        if (lane < 2) STSC(gran_f32(lane ? Ls : Ms, tag), rws, WS_ML + blockIdx.x * 128 + lane * 16);
        u32x4 gq;
#pragma unroll 1
        for (unsigned spins = 0; spins < SPIN_CAP; ++spins) {
          asm volatile("" ::: "memory");
          gq = LDSC(rws, WS_ML + (blockIdx.x ^ 16) * 128 + (lane & 1) * 16, 0);
          if (__all(gran_f32_ok(gq, tag))) break;
          __builtin_amdgcn_s_sleep(8);
        }
        const float gv = gran_f32_val(gq), go = dpp<XOR1>(gv);
        const float Mp = (lane & 1) ? go : gv, Lp = (lane & 1) ? gv : go;
        const float M = fmaxf(Ms, Mp);
        const float L = Ls * __builtin_amdgcn_exp2f((Ms - M) * C2) + Lp * __builtin_amdgcn_exp2f((Mp - M) * C2);
        if (lane < 4) lds[L_FAC + lane] = gate * __builtin_amdgcn_exp2f((mw - M) * C2) / L;
      }
    }
#pragma unroll
    for (int i = 0; i < 32; ++i) { PRE(76 + i); wo[i] = LDNT(rwo, lane56, (32 * w + i) * (DM * 2)); TRK(76 + i); }
    SB();
#pragma unroll
    for (int i = 0; i < 16; ++i) pair_perm(wo[2 * i], wo[2 * i + 1]);
    PIN8(y);
    if (blockIdx.x == 0 && tid == 256) { cnt[9] = (unsigned)((epoch + 1ull) >> 32); cnt[10] = (unsigned)(epoch + 1ull); }
  }
  const int zcol = 14 * (tid >> 4) + (tid & 15);
  const int zst = WS_Z + ((b * 32 + (tid >> 4)) * 32 + j) * 256 + (tid & 15) * 16;
  const int zld = WS_Z + (b * 32 + j) * 8192 + tid * 16;
  wg_barrier();
  {
    const int wp = (lane >> 3) & 3;
    f32x4 o4 = *(const f32x4*)(lds + L_OP + wp * 256 + 32 * w + 4 * (lane & 7));
    const float fc = lds[L_FAC + wp];
#pragma unroll
    for (int k = 0; k < 4; ++k) { float t = o4[k] * fc; t += dpp<XOR8>(t); o4[k] = swap16_add(t, t); }
    const unsigned d0 = cvtpk(o4[0], o4[1]), d1 = cvtpk(o4[2], o4[3]);
#pragma unroll
    for (int i = 0; i < 16; ++i) {
      const unsigned op = __builtin_amdgcn_readlane((i & 1) ? d1 : d0, i >> 1);
#pragma unroll
      for (int k = 0; k < 4; ++k) { y[k] = dot2(wo[2 * i][k], op, y[k]); y[4 + k] = dot2(wo[2 * i + 1][k], op, y[4 + k]); }
    }
    if (lane < 56) { float* yp = lds + L_YP + w * 448 + lane * 8; *(f32x4*)yp = f32x4{y[0], y[1], y[2], y[3]}; *(f32x4*)(yp + 4) = f32x4{y[4], y[5], y[6], y[7]}; }
  }
  wg_barrier();
  {
    float z = 0.f;
    if ((tid & 15) < 14) {
#pragma unroll
      for (int i = 0; i < 8; ++i) z += lds[L_YP + i * 448 + zcol];
      STSC(gran_f32(z, tag), rws, zst);
    }
  }
  {
    float zv = 0.f;
    bool done = (tid & 15) >= 14;
    __builtin_amdgcn_s_sleep(PINIT);
#pragma unroll 1
    for (unsigned spins = 0; spins < SPIN_CAP; ++spins) {
      asm volatile("" ::: "memory");
      if (!done) { const u32x4 g = LDSC(rws, zld, 0); if (gran_f32_ok(g, tag)) { zv = gran_f32_val(g); done = true; } }
      if (__all(done)) break;
      __builtin_amdgcn_s_sleep(PGAP);
    }
    zv = swap16_add(zv, zv); zv = swap32_add(zv, zv);
    if (lane < 16) lds[L_FIN + w * 16 + lane] = zv;
  }
  wg_barrier();
  if (tid < 14) {
    float o = 0.f;
#pragma unroll
    for (int i = 0; i < 8; ++i) o += lds[L_FIN + i * 16 + tid];
    p.out[b * DM + 14 * j + tid] = (u16)(cvtpk(o, 0.f) & 0xffffu);
  }
  if (blockIdx.x == 0 && tid == 64) __hip_atomic_store(wsq + WS_EPOCH / 8, ((u64)cnt[9] << 32) | cnt[10], RLX_AGENT);
}

extern "C" void kernel_launch(void* const* d_in, const int* in_sizes, int n_in, void* d_out, int out_size, void* d_ws, size_t ws_size, hipStream_t stream) {
  Params p;
  p.x = (const u16*)d_in[0]; p.kc = (const u16*)d_in[1]; p.vc = (const u16*)d_in[2]; p.wq = (const u16*)d_in[3]; p.wo = (const u16*)d_in[4];
  p.wg = (const u16*)d_in[6]; p.wu = (const u16*)d_in[7]; p.wd = (const u16*)d_in[8]; p.gw = (const u16*)d_in[9];
  p.out = (u16*)d_out; p.ws = (unsigned char*)d_ws;
  fused_kernel<<<256, 512, 0, stream>>>(p);
}
```
